```python
import torch

def _softmax(x, axis=-1):
    return torch.softmax(x, dim=axis)
def _gelu(x):
    return torch.nn.functional.gelu(x, approximate="tanh")
def _silu(x):
    return torch.nn.functional.silu(x)
def _sigmoid(x):
    return torch.sigmoid(x)
def _layernorm(x, axis=-1, eps=1e-5):
    m = x.mean(dim=axis, keepdim=True)
    v = x.var(dim=axis, keepdim=True, unbiased=False)
    return (x - m) / torch.sqrt(v + eps)
def _scatter_add(tgt, idx, src, axis=0):
    return tgt.clone().index_add_(axis, idx, src)
def _segment_sum(src, seg, n):
    out = torch.zeros((n,) + src.shape[1:], dtype=src.dtype)
    return out.index_add_(0, seg, src)
def _pad(x, pw):
    flat = tuple(v for pair in reversed(pw) for v in pair)
    return torch.nn.functional.pad(x, flat)

kernel_name = 'parallel_sum__spectral_gfnet__glu_gated_geglu__prompt'

def setup_inputs():

    _w = (lambda a: (lambda t: t.to(torch.bfloat16) if t.is_floating_point() else t)(torch.as_tensor(a)))
    def _A_setup():
        B, S, D = 1, 2048, 448
        Df = D // 2 + 1
        return {
            "x": _w((lambda *s: torch.randn(*s, dtype=torch.bfloat16) * 0.05)(B, S, D)),
            "w_r": _w((lambda *s: torch.randn(*s, dtype=torch.bfloat16) * 0.8)(S, Df) * 0.02),
            "w_i": _w((lambda *s: torch.randn(*s, dtype=torch.bfloat16) * 0.8)(S, Df) * 0.02),
        }
    _A_in = _A_setup()

    def _B_setup():
        B, S, Dm, Df = 1, 2048, 448, 1792
        return {
            "x": _w((lambda *s: torch.randn(*s, dtype=torch.bfloat16) * 0.05)(B, S, Dm)),
            "w_gate": _w((lambda *s: torch.randn(*s, dtype=torch.bfloat16) * 0.4)(Dm, Df) * (Dm ** -0.5)),
            "w_up": _w((lambda *s: torch.randn(*s, dtype=torch.bfloat16) * 0.4)(Dm, Df) * (Dm ** -0.5)),
            "w_down": _w((lambda *s: torch.randn(*s, dtype=torch.bfloat16) * 0.4)(Df, Dm) * (Df ** -0.5)),
        }
    _B_in = _B_setup()

    return {
        "A_x": _A_in["x"],
        "A_w_r": _A_in["w_r"],
        "A_w_i": _A_in["w_i"],
        "B_x": _B_in["x"],
        "B_w_gate": _B_in["w_gate"],
        "B_w_up": _B_in["w_up"],
        "B_w_down": _B_in["w_down"],
    }

def reference(A_x, A_w_r, A_w_i, B_x, B_w_gate, B_w_up, B_w_down):
    def _A_ref(x, w_r, w_i):
        B, S, D = x.shape
        X = (lambda x, axes=(-2,-1): torch.fft.rfft2(x.float(), dim=axes))(x, axes=(-2, -1))
        W = w_r + 1j * w_i
        X = X * W
        y = (lambda x, s, axes=(-2,-1): torch.fft.irfft2(x, s=s, dim=axes).to(torch.bfloat16))(X, (S, D), axes=(-2, -1))
        return y

    def _B_ref(x, w_gate, w_up, w_down):
        g = torch.einsum("bsd,df->bsf", x, w_gate)
        u = torch.einsum("bsd,df->bsf", x, w_up)
        h = _gelu(g) * u
        return torch.einsum("bsf,fd->bsd", h, w_down)

    x = A_x
    xn = _layernorm(x, axis=-1)
    return x + _A_ref(xn, A_w_r, A_w_i) + _B_ref(xn, B_w_gate, B_w_up, B_w_down)
```

```cpp
#include <hip/hip_runtime.h>
#include <hip/hip_bf16.h>
#include <stdint.h>

typedef unsigned short u16;
typedef unsigned int u32;
using bf16x8 = __attribute__((ext_vector_type(8))) short;
using f32x2 = __attribute__((ext_vector_type(2))) float;
using f32x4 = __attribute__((ext_vector_type(4))) float;
using f32x16 = __attribute__((ext_vector_type(16))) float;
using u32x2 = __attribute__((ext_vector_type(2))) unsigned;
using u32x4 = __attribute__((ext_vector_type(4))) unsigned;
using bf16x2 = __attribute__((ext_vector_type(2))) __bf16;
#if defined(__HIP_DEVICE_COMPILE__)
#define NOPK __attribute__((target("no-packed-fp32-ops")))
#else
#define NOPK
#endif
#define DI NOPK __device__ __forceinline__
#define STAMP(kid, idx)

__device__ const float TW2048[4096] = {
0x1.0000000000000p+0f,0.0f,0x1.ffff620000000p-1f,-0x1.921f8c0000000p-9f,0x1.fffd880000000p-1f,-0x1.921f100000000p-8f,0x1.fffa720000000p-1f,-0x1.2d96b00000000p-7f,
0x1.fff6220000000p-1f,-0x1.921d200000000p-7f,0x1.fff0940000000p-1f,-0x1.f6a2960000000p-7f,0x1.ffe9cc0000000p-1f,-0x1.2d936c0000000p-6f,0x1.ffe1c60000000p-1f,-0x1.5fd4d20000000p-6f,
0x1.ffd8860000000p-1f,-0x1.9215600000000p-6f,0x1.ffce0a0000000p-1f,-0x1.c454f40000000p-6f,0x1.ffc2520000000p-1f,-0x1.f693740000000p-6f,0x1.ffb55e0000000p-1f,-0x1.14685e0000000p-5f,
0x1.ffa72e0000000p-1f,-0x1.2d86580000000p-5f,0x1.ff97c40000000p-1f,-0x1.46a3960000000p-5f,0x1.ff871e0000000p-1f,-0x1.5fc00e0000000p-5f,0x1.ff753c0000000p-1f,-0x1.78dbaa0000000p-5f,
0x1.ff621e0000000p-1f,-0x1.91f6600000000p-5f,0x1.ff4dc60000000p-1f,-0x1.ab101c0000000p-5f,0x1.ff38300000000p-1f,-0x1.c428d20000000p-5f,0x1.ff21620000000p-1f,-0x1.dd40700000000p-5f,
0x1.ff09560000000p-1f,-0x1.f656e80000000p-5f,0x1.fef0100000000p-1f,-0x1.07b6140000000p-4f,0x1.fed58e0000000p-1f,-0x1.1440140000000p-4f,0x1.feb9d20000000p-1f,-0x1.20c9680000000p-4f,
0x1.fe9cda0000000p-1f,-0x1.2d520a0000000p-4f,0x1.fe7ea80000000p-1f,-0x1.39d9f20000000p-4f,0x1.fe5f3a0000000p-1f,-0x1.4661180000000p-4f,0x1.fe3e920000000p-1f,-0x1.52e7740000000p-4f,
0x1.fe1cb00000000p-1f,-0x1.5f6d000000000p-4f,0x1.fdf9920000000p-1f,-0x1.6bf1b40000000p-4f,0x1.fdd53a0000000p-1f,-0x1.7875860000000p-4f,0x1.fdafa80000000p-1f,-0x1.84f8720000000p-4f,
0x1.fd88da0000000p-1f,-0x1.917a6c0000000p-4f,0x1.fd60d20000000p-1f,-0x1.9dfb6e0000000p-4f,0x1.fd37920000000p-1f,-0x1.aa7b720000000p-4f,0x1.fd0d160000000p-1f,-0x1.b6fa6e0000000p-4f,
0x1.fce1600000000p-1f,-0x1.c3785c0000000p-4f,0x1.fcb4700000000p-1f,-0x1.cff5340000000p-4f,0x1.fc86460000000p-1f,-0x1.dc70ec0000000p-4f,0x1.fc56e40000000p-1f,-0x1.e8eb800000000p-4f,
0x1.fc26480000000p-1f,-0x1.f564e60000000p-4f,0x1.fbf4700000000p-1f,-0x1.00ee8a0000000p-3f,0x1.fbc1620000000p-1f,-0x1.072a040000000p-3f,0x1.fb8d180000000p-1f,-0x1.0d64dc0000000p-3f,
0x1.fb57980000000p-1f,-0x1.139f0c0000000p-3f,0x1.fb20dc0000000p-1f,-0x1.19d8940000000p-3f,0x1.fae8e80000000p-1f,-0x1.20116e0000000p-3f,0x1.faafbc0000000p-1f,-0x1.2649940000000p-3f,
0x1.fa75580000000p-1f,-0x1.2c81060000000p-3f,0x1.fa39ba0000000p-1f,-0x1.32b7c00000000p-3f,0x1.f9fce60000000p-1f,-0x1.38edbc0000000p-3f,0x1.f9bed80000000p-1f,-0x1.3f22f60000000p-3f,
0x1.f97f920000000p-1f,-0x1.45576c0000000p-3f,0x1.f93f140000000p-1f,-0x1.4b8b180000000p-3f,0x1.f8fd600000000p-1f,-0x1.51bdf80000000p-3f,0x1.f8ba740000000p-1f,-0x1.57f0080000000p-3f,
0x1.f876500000000p-1f,-0x1.5e21440000000p-3f,0x1.f830f40000000p-1f,-0x1.6451a80000000p-3f,0x1.f7ea620000000p-1f,-0x1.6a81300000000p-3f,0x1.f7a29a0000000p-1f,-0x1.70afd80000000p-3f,
0x1.f7599a0000000p-1f,-0x1.76dd9e0000000p-3f,0x1.f70f640000000p-1f,-0x1.7d0a7c0000000p-3f,0x1.f6c3f80000000p-1f,-0x1.83366e0000000p-3f,0x1.f677560000000p-1f,-0x1.8961720000000p-3f,
0x1.f6297c0000000p-1f,-0x1.8f8b840000000p-3f,0x1.f5da6e0000000p-1f,-0x1.95b49e0000000p-3f,0x1.f58a2c0000000p-1f,-0x1.9bdcc00000000p-3f,0x1.f538b20000000p-1f,-0x1.a203e20000000p-3f,
0x1.f4e6040000000p-1f,-0x1.a82a020000000p-3f,0x1.f492200000000p-1f,-0x1.ae4f1e0000000p-3f,0x1.f43d080000000p-1f,-0x1.b4732e0000000p-3f,0x1.f3e6bc0000000p-1f,-0x1.ba96340000000p-3f,
0x1.f38f3a0000000p-1f,-0x1.c0b8260000000p-3f,0x1.f336860000000p-1f,-0x1.c6d9060000000p-3f,0x1.f2dc9c0000000p-1f,-0x1.ccf8cc0000000p-3f,0x1.f281800000000p-1f,-0x1.d317740000000p-3f,
0x1.f225300000000p-1f,-0x1.d934fe0000000p-3f,0x1.f1c7ac0000000p-1f,-0x1.df51640000000p-3f,0x1.f168f60000000p-1f,-0x1.e56ca20000000p-3f,0x1.f1090c0000000p-1f,-0x1.eb86b40000000p-3f,
0x1.f0a7f00000000p-1f,-0x1.f19f980000000p-3f,0x1.f045a20000000p-1f,-0x1.f7b7480000000p-3f,0x1.efe2200000000p-1f,-0x1.fdcdc20000000p-3f,0x1.ef7d6e0000000p-1f,-0x1.01f1800000000p-2f,
0x1.ef178a0000000p-1f,-0x1.04fb800000000p-2f,0x1.eeb0740000000p-1f,-0x1.0804e00000000p-2f,0x1.ee482e0000000p-1f,-0x1.0b0d9c0000000p-2f,0x1.eddeb60000000p-1f,-0x1.0e15b40000000p-2f,
0x1.ed740e0000000p-1f,-0x1.111d260000000p-2f,0x1.ed08360000000p-1f,-0x1.1423ee0000000p-2f,0x1.ec9b2e0000000p-1f,-0x1.172a0e0000000p-2f,0x1.ec2cf40000000p-1f,-0x1.1a2f800000000p-2f,
0x1.ebbd8c0000000p-1f,-0x1.1d34440000000p-2f,0x1.eb4cf60000000p-1f,-0x1.2038580000000p-2f,0x1.eadb2e0000000p-1f,-0x1.233bba0000000p-2f,0x1.ea683a0000000p-1f,-0x1.263e6a0000000p-2f,
0x1.e9f4160000000p-1f,-0x1.2940620000000p-2f,0x1.e97ec40000000p-1f,-0x1.2c41a40000000p-2f,0x1.e908440000000p-1f,-0x1.2f422e0000000p-2f,0x1.e890960000000p-1f,-0x1.3241fc0000000p-2f,
0x1.e817ba0000000p-1f,-0x1.35410c0000000p-2f,0x1.e79db20000000p-1f,-0x1.383f5e0000000p-2f,0x1.e7227e0000000p-1f,-0x1.3b3cf00000000p-2f,0x1.e6a61c0000000p-1f,-0x1.3e39be0000000p-2f,
0x1.e6288e0000000p-1f,-0x1.4135ca0000000p-2f,0x1.e5a9d60000000p-1f,-0x1.44310e0000000p-2f,0x1.e529f00000000p-1f,-0x1.472b8a0000000p-2f,0x1.e4a8e00000000p-1f,-0x1.4a253e0000000p-2f,
0x1.e426a40000000p-1f,-0x1.4d1e240000000p-2f,0x1.e3a33e0000000p-1f,-0x1.50163e0000000p-2f,0x1.e31eae0000000p-1f,-0x1.530d880000000p-2f,0x1.e298f40000000p-1f,-0x1.5604020000000p-2f,
0x1.e212100000000p-1f,-0x1.58f9a80000000p-2f,0x1.e18a020000000p-1f,-0x1.5bee780000000p-2f,0x1.e100cc0000000p-1f,-0x1.5ee2740000000p-2f,0x1.e0766e0000000p-1f,-0x1.61d5960000000p-2f,
0x1.dfeae60000000p-1f,-0x1.64c7de0000000p-2f,0x1.df5e360000000p-1f,-0x1.67b94a0000000p-2f,0x1.ded0600000000p-1f,-0x1.6aa9d80000000p-2f,0x1.de41600000000p-1f,-0x1.6d99860000000p-2f,
0x1.ddb13c0000000p-1f,-0x1.7088540000000p-2f,0x1.dd1ff00000000p-1f,-0x1.73763c0000000p-2f,0x1.dc8d7c0000000p-1f,-0x1.7663400000000p-2f,0x1.dbf9e40000000p-1f,-0x1.794f5e0000000p-2f,
0x1.db65260000000p-1f,-0x1.7c3a940000000p-2f,0x1.dacf420000000p-1f,-0x1.7f24de0000000p-2f,0x1.da383a0000000p-1f,-0x1.820e3c0000000p-2f,0x1.d9a00e0000000p-1f,-0x1.84f6aa0000000p-2f,
0x1.d906bc0000000p-1f,-0x1.87de2a0000000p-2f,0x1.d86c480000000p-1f,-0x1.8ac4b80000000p-2f,0x1.d7d0b00000000p-1f,-0x1.8daa520000000p-2f,0x1.d733f60000000p-1f,-0x1.908ef80000000p-2f,
0x1.d696180000000p-1f,-0x1.9372a60000000p-2f,0x1.d5f7180000000p-1f,-0x1.96555c0000000p-2f,0x1.d556f60000000p-1f,-0x1.9937160000000p-2f,0x1.d4b5b20000000p-1f,-0x1.9c17d40000000p-2f,
0x1.d4134e0000000p-1f,-0x1.9ef7940000000p-2f,0x1.d36fc80000000p-1f,-0x1.a1d6540000000p-2f,0x1.d2cb220000000p-1f,-0x1.a4b4120000000p-2f,0x1.d2255c0000000p-1f,-0x1.a790ce0000000p-2f,
0x1.d17e780000000p-1f,-0x1.aa6c820000000p-2f,0x1.d0d6720000000p-1f,-0x1.ad47320000000p-2f,0x1.d02d500000000p-1f,-0x1.b020d60000000p-2f,0x1.cf830e0000000p-1f,-0x1.b2f9720000000p-2f,
0x1.ced7b00000000p-1f,-0x1.b5d1000000000p-2f,0x1.ce2b320000000p-1f,-0x1.b8a7820000000p-2f,0x1.cd7d980000000p-1f,-0x1.bb7cf20000000p-2f,0x1.cccee20000000p-1f,-0x1.be51520000000p-2f,
0x1.cc1f100000000p-1f,-0x1.c1249e0000000p-2f,0x1.cb6e200000000p-1f,-0x1.c3f6d40000000p-2f,0x1.cabc160000000p-1f,-0x1.c6c7f40000000p-2f,0x1.ca08f20000000p-1f,-0x1.c997fc0000000p-2f,
0x1.c954b20000000p-1f,-0x1.cc66ea0000000p-2f,0x1.c89f580000000p-1f,-0x1.cf34ba0000000p-2f,0x1.c7e8e60000000p-1f,-0x1.d2016e0000000p-2f,0x1.c731580000000p-1f,-0x1.d4cd020000000p-2f,
0x1.c678b40000000p-1f,-0x1.d797760000000p-2f,0x1.c5bef60000000p-1f,-0x1.da60c60000000p-2f,0x1.c504200000000p-1f,-0x1.dd28f20000000p-2f,0x1.c448340000000p-1f,-0x1.dfeff60000000p-2f,
0x1.c38b300000000p-1f,-0x1.e2b5d40000000p-2f,0x1.c2cd140000000p-1f,-0x1.e57a860000000p-2f,0x1.c20de40000000p-1f,-0x1.e83e0e0000000p-2f,0x1.c14d9e0000000p-1f,-0x1.eb006a0000000p-2f,
0x1.c08c420000000p-1f,-0x1.edc1960000000p-2f,0x1.bfc9d20000000p-1f,-0x1.f081900000000p-2f,0x1.bf064e0000000p-1f,-0x1.f3405a0000000p-2f,0x1.be41b60000000p-1f,-0x1.f5fdee0000000p-2f,
0x1.bd7c0a0000000p-1f,-0x1.f8ba4e0000000p-2f,0x1.bcb54c0000000p-1f,-0x1.fb75760000000p-2f,0x1.bbed7c0000000p-1f,-0x1.fe2f640000000p-2f,0x1.bb249a0000000p-1f,-0x1.00740c0000000p-1f,
0x1.ba5aa60000000p-1f,-0x1.01cfc80000000p-1f,0x1.b98fa20000000p-1f,-0x1.032ae60000000p-1f,0x1.b8c38e0000000p-1f,-0x1.0485620000000p-1f,0x1.b7f6680000000p-1f,-0x1.05df3e0000000p-1f,
0x1.b728340000000p-1f,-0x1.07387a0000000p-1f,0x1.b658f20000000p-1f,-0x1.0891120000000p-1f,0x1.b588a00000000p-1f,-0x1.09e9080000000p-1f,0x1.b4b7400000000p-1f,-0x1.0b40580000000p-1f,
0x1.b3e4d40000000p-1f,-0x1.0c97040000000p-1f,0x1.b3115a0000000p-1f,-0x1.0ded0c0000000p-1f,0x1.b23cd40000000p-1f,-0x1.0f426c0000000p-1f,0x1.b167420000000p-1f,-0x1.1097240000000p-1f,
0x1.b090a60000000p-1f,-0x1.11eb360000000p-1f,0x1.afb8fe0000000p-1f,-0x1.133e9c0000000p-1f,0x1.aee04c0000000p-1f,-0x1.14915a0000000p-1f,0x1.ae06900000000p-1f,-0x1.15e36e0000000p-1f,
0x1.ad2bca0000000p-1f,-0x1.1734d60000000p-1f,0x1.ac4ffc0000000p-1f,-0x1.1885920000000p-1f,0x1.ab73260000000p-1f,-0x1.19d5a00000000p-1f,0x1.aa95480000000p-1f,-0x1.1b25020000000p-1f,
0x1.a9b6620000000p-1f,-0x1.1c73b40000000p-1f,0x1.a8d6760000000p-1f,-0x1.1dc1b60000000p-1f,0x1.a7f5860000000p-1f,-0x1.1f0f080000000p-1f,0x1.a7138e0000000p-1f,-0x1.205baa0000000p-1f,
0x1.a630920000000p-1f,-0x1.21a79a0000000p-1f,0x1.a54c920000000p-1f,-0x1.22f2d60000000p-1f,0x1.a4678c0000000p-1f,-0x1.243d600000000p-1f,0x1.a381840000000p-1f,-0x1.2587340000000p-1f,
0x1.a29a7a0000000p-1f,-0x1.26d0540000000p-1f,0x1.a1b26e0000000p-1f,-0x1.2818be0000000p-1f,0x1.a0c95e0000000p-1f,-0x1.2960720000000p-1f,0x1.9fdf500000000p-1f,-0x1.2aa76e0000000p-1f,
0x1.9ef43e0000000p-1f,-0x1.2bedb20000000p-1f,0x1.9e082e0000000p-1f,-0x1.2d333e0000000p-1f,0x1.9d1b200000000p-1f,-0x1.2e780e0000000p-1f,0x1.9c2d120000000p-1f,-0x1.2fbc240000000p-1f,
0x1.9b3e040000000p-1f,-0x1.30ff800000000p-1f,0x1.9a4dfa0000000p-1f,-0x1.32421e0000000p-1f,0x1.995cf20000000p-1f,-0x1.3384000000000p-1f,0x1.986af00000000p-1f,-0x1.34c5260000000p-1f,
0x1.9777f00000000p-1f,-0x1.36058c0000000p-1f,0x1.9683f40000000p-1f,-0x1.3745320000000p-1f,0x1.958efe0000000p-1f,-0x1.3884180000000p-1f,0x1.94990e0000000p-1f,-0x1.39c23e0000000p-1f,
0x1.93a2240000000p-1f,-0x1.3affa20000000p-1f,0x1.92aa420000000p-1f,-0x1.3c3c440000000p-1f,0x1.91b1660000000p-1f,-0x1.3d78240000000p-1f,0x1.90b7940000000p-1f,-0x1.3eb33e0000000p-1f,
0x1.8fbcca0000000p-1f,-0x1.3fed960000000p-1f,0x1.8ec10a0000000p-1f,-0x1.4127260000000p-1f,0x1.8dc4540000000p-1f,-0x1.425ff20000000p-1f,0x1.8cc6a80000000p-1f,-0x1.4397f60000000p-1f,
0x1.8bc8060000000p-1f,-0x1.44cf320000000p-1f,0x1.8ac8720000000p-1f,-0x1.4605a60000000p-1f,0x1.89c7ea0000000p-1f,-0x1.473b520000000p-1f,0x1.88c66e0000000p-1f,-0x1.4870340000000p-1f,
0x1.87c4000000000p-1f,-0x1.49a44a0000000p-1f,0x1.86c0a20000000p-1f,-0x1.4ad7960000000p-1f,0x1.85bc520000000p-1f,-0x1.4c0a140000000p-1f,0x1.84b7120000000p-1f,-0x1.4d3bc60000000p-1f,
0x1.83b0e00000000p-1f,-0x1.4e6cac0000000p-1f,0x1.82a9c20000000p-1f,-0x1.4f9cc20000000p-1f,0x1.81a1b40000000p-1f,-0x1.50cc0a0000000p-1f,0x1.8098b80000000p-1f,-0x1.51fa820000000p-1f,
0x1.7f8ece0000000p-1f,-0x1.53282a0000000p-1f,0x1.7e83f80000000p-1f,-0x1.5455000000000p-1f,0x1.7d78360000000p-1f,-0x1.5581040000000p-1f,0x1.7c6b8a0000000p-1f,-0x1.56ac360000000p-1f,
0x1.7b5df20000000p-1f,-0x1.57d6940000000p-1f,0x1.7a4f700000000p-1f,-0x1.59001e0000000p-1f,0x1.7940060000000p-1f,-0x1.5a28d20000000p-1f,0x1.782fb20000000p-1f,-0x1.5b50b20000000p-1f,
0x1.771e760000000p-1f,-0x1.5c77bc0000000p-1f,0x1.760c520000000p-1f,-0x1.5d9dee0000000p-1f,0x1.74f9480000000p-1f,-0x1.5ec34a0000000p-1f,0x1.73e5580000000p-1f,-0x1.5fe7cc0000000p-1f,
0x1.72d0840000000p-1f,-0x1.610b760000000p-1f,0x1.71baca0000000p-1f,-0x1.622e440000000p-1f,0x1.70a42c0000000p-1f,-0x1.63503a0000000p-1f,0x1.6f8caa0000000p-1f,-0x1.6471540000000p-1f,
0x1.6e74460000000p-1f,-0x1.6591920000000p-1f,0x1.6d5afe0000000p-1f,-0x1.66b0f40000000p-1f,0x1.6c40d80000000p-1f,-0x1.67cf780000000p-1f,0x1.6b25ce0000000p-1f,-0x1.68ed1e0000000p-1f,
0x1.6a09e60000000p-1f,-0x1.6a09e60000000p-1f,0x1.68ed1e0000000p-1f,-0x1.6b25ce0000000p-1f,0x1.67cf780000000p-1f,-0x1.6c40d80000000p-1f,0x1.66b0f40000000p-1f,-0x1.6d5afe0000000p-1f,
0x1.6591920000000p-1f,-0x1.6e74460000000p-1f,0x1.6471540000000p-1f,-0x1.6f8caa0000000p-1f,0x1.63503a0000000p-1f,-0x1.70a42c0000000p-1f,0x1.622e440000000p-1f,-0x1.71baca0000000p-1f,
0x1.610b760000000p-1f,-0x1.72d0840000000p-1f,0x1.5fe7cc0000000p-1f,-0x1.73e5580000000p-1f,0x1.5ec34a0000000p-1f,-0x1.74f9480000000p-1f,0x1.5d9dee0000000p-1f,-0x1.760c520000000p-1f,
0x1.5c77bc0000000p-1f,-0x1.771e760000000p-1f,0x1.5b50b20000000p-1f,-0x1.782fb20000000p-1f,0x1.5a28d20000000p-1f,-0x1.7940060000000p-1f,0x1.59001e0000000p-1f,-0x1.7a4f700000000p-1f,
0x1.57d6940000000p-1f,-0x1.7b5df20000000p-1f,0x1.56ac360000000p-1f,-0x1.7c6b8a0000000p-1f,0x1.5581040000000p-1f,-0x1.7d78360000000p-1f,0x1.5455000000000p-1f,-0x1.7e83f80000000p-1f,
0x1.53282a0000000p-1f,-0x1.7f8ece0000000p-1f,0x1.51fa820000000p-1f,-0x1.8098b80000000p-1f,0x1.50cc0a0000000p-1f,-0x1.81a1b40000000p-1f,0x1.4f9cc20000000p-1f,-0x1.82a9c20000000p-1f,
0x1.4e6cac0000000p-1f,-0x1.83b0e00000000p-1f,0x1.4d3bc60000000p-1f,-0x1.84b7120000000p-1f,0x1.4c0a140000000p-1f,-0x1.85bc520000000p-1f,0x1.4ad7960000000p-1f,-0x1.86c0a20000000p-1f,
0x1.49a44a0000000p-1f,-0x1.87c4000000000p-1f,0x1.4870340000000p-1f,-0x1.88c66e0000000p-1f,0x1.473b520000000p-1f,-0x1.89c7ea0000000p-1f,0x1.4605a60000000p-1f,-0x1.8ac8720000000p-1f,
0x1.44cf320000000p-1f,-0x1.8bc8060000000p-1f,0x1.4397f60000000p-1f,-0x1.8cc6a80000000p-1f,0x1.425ff20000000p-1f,-0x1.8dc4540000000p-1f,0x1.4127260000000p-1f,-0x1.8ec10a0000000p-1f,
0x1.3fed960000000p-1f,-0x1.8fbcca0000000p-1f,0x1.3eb33e0000000p-1f,-0x1.90b7940000000p-1f,0x1.3d78240000000p-1f,-0x1.91b1660000000p-1f,0x1.3c3c440000000p-1f,-0x1.92aa420000000p-1f,
0x1.3affa20000000p-1f,-0x1.93a2240000000p-1f,0x1.39c23e0000000p-1f,-0x1.94990e0000000p-1f,0x1.3884180000000p-1f,-0x1.958efe0000000p-1f,0x1.3745320000000p-1f,-0x1.9683f40000000p-1f,
0x1.36058c0000000p-1f,-0x1.9777f00000000p-1f,0x1.34c5260000000p-1f,-0x1.986af00000000p-1f,0x1.3384000000000p-1f,-0x1.995cf20000000p-1f,0x1.32421e0000000p-1f,-0x1.9a4dfa0000000p-1f,
0x1.30ff800000000p-1f,-0x1.9b3e040000000p-1f,0x1.2fbc240000000p-1f,-0x1.9c2d120000000p-1f,0x1.2e780e0000000p-1f,-0x1.9d1b200000000p-1f,0x1.2d333e0000000p-1f,-0x1.9e082e0000000p-1f,
0x1.2bedb20000000p-1f,-0x1.9ef43e0000000p-1f,0x1.2aa76e0000000p-1f,-0x1.9fdf500000000p-1f,0x1.2960720000000p-1f,-0x1.a0c95e0000000p-1f,0x1.2818be0000000p-1f,-0x1.a1b26e0000000p-1f,
0x1.26d0540000000p-1f,-0x1.a29a7a0000000p-1f,0x1.2587340000000p-1f,-0x1.a381840000000p-1f,0x1.243d600000000p-1f,-0x1.a4678c0000000p-1f,0x1.22f2d60000000p-1f,-0x1.a54c920000000p-1f,
0x1.21a79a0000000p-1f,-0x1.a630920000000p-1f,0x1.205baa0000000p-1f,-0x1.a7138e0000000p-1f,0x1.1f0f080000000p-1f,-0x1.a7f5860000000p-1f,0x1.1dc1b60000000p-1f,-0x1.a8d6760000000p-1f,
0x1.1c73b40000000p-1f,-0x1.a9b6620000000p-1f,0x1.1b25020000000p-1f,-0x1.aa95480000000p-1f,0x1.19d5a00000000p-1f,-0x1.ab73260000000p-1f,0x1.1885920000000p-1f,-0x1.ac4ffc0000000p-1f,
0x1.1734d60000000p-1f,-0x1.ad2bca0000000p-1f,0x1.15e36e0000000p-1f,-0x1.ae06900000000p-1f,0x1.14915a0000000p-1f,-0x1.aee04c0000000p-1f,0x1.133e9c0000000p-1f,-0x1.afb8fe0000000p-1f,
0x1.11eb360000000p-1f,-0x1.b090a60000000p-1f,0x1.1097240000000p-1f,-0x1.b167420000000p-1f,0x1.0f426c0000000p-1f,-0x1.b23cd40000000p-1f,0x1.0ded0c0000000p-1f,-0x1.b3115a0000000p-1f,
0x1.0c97040000000p-1f,-0x1.b3e4d40000000p-1f,0x1.0b40580000000p-1f,-0x1.b4b7400000000p-1f,0x1.09e9080000000p-1f,-0x1.b588a00000000p-1f,0x1.0891120000000p-1f,-0x1.b658f20000000p-1f,
0x1.07387a0000000p-1f,-0x1.b728340000000p-1f,0x1.05df3e0000000p-1f,-0x1.b7f6680000000p-1f,0x1.0485620000000p-1f,-0x1.b8c38e0000000p-1f,0x1.032ae60000000p-1f,-0x1.b98fa20000000p-1f,
0x1.01cfc80000000p-1f,-0x1.ba5aa60000000p-1f,0x1.00740c0000000p-1f,-0x1.bb249a0000000p-1f,0x1.fe2f640000000p-2f,-0x1.bbed7c0000000p-1f,0x1.fb75760000000p-2f,-0x1.bcb54c0000000p-1f,
0x1.f8ba4e0000000p-2f,-0x1.bd7c0a0000000p-1f,0x1.f5fdee0000000p-2f,-0x1.be41b60000000p-1f,0x1.f3405a0000000p-2f,-0x1.bf064e0000000p-1f,0x1.f081900000000p-2f,-0x1.bfc9d20000000p-1f,
0x1.edc1960000000p-2f,-0x1.c08c420000000p-1f,0x1.eb006a0000000p-2f,-0x1.c14d9e0000000p-1f,0x1.e83e0e0000000p-2f,-0x1.c20de40000000p-1f,0x1.e57a860000000p-2f,-0x1.c2cd140000000p-1f,
0x1.e2b5d40000000p-2f,-0x1.c38b300000000p-1f,0x1.dfeff60000000p-2f,-0x1.c448340000000p-1f,0x1.dd28f20000000p-2f,-0x1.c504200000000p-1f,0x1.da60c60000000p-2f,-0x1.c5bef60000000p-1f,
0x1.d797760000000p-2f,-0x1.c678b40000000p-1f,0x1.d4cd020000000p-2f,-0x1.c731580000000p-1f,0x1.d2016e0000000p-2f,-0x1.c7e8e60000000p-1f,0x1.cf34ba0000000p-2f,-0x1.c89f580000000p-1f,
0x1.cc66ea0000000p-2f,-0x1.c954b20000000p-1f,0x1.c997fc0000000p-2f,-0x1.ca08f20000000p-1f,0x1.c6c7f40000000p-2f,-0x1.cabc160000000p-1f,0x1.c3f6d40000000p-2f,-0x1.cb6e200000000p-1f,
0x1.c1249e0000000p-2f,-0x1.cc1f100000000p-1f,0x1.be51520000000p-2f,-0x1.cccee20000000p-1f,0x1.bb7cf20000000p-2f,-0x1.cd7d980000000p-1f,0x1.b8a7820000000p-2f,-0x1.ce2b320000000p-1f,
0x1.b5d1000000000p-2f,-0x1.ced7b00000000p-1f,0x1.b2f9720000000p-2f,-0x1.cf830e0000000p-1f,0x1.b020d60000000p-2f,-0x1.d02d500000000p-1f,0x1.ad47320000000p-2f,-0x1.d0d6720000000p-1f,
0x1.aa6c820000000p-2f,-0x1.d17e780000000p-1f,0x1.a790ce0000000p-2f,-0x1.d2255c0000000p-1f,0x1.a4b4120000000p-2f,-0x1.d2cb220000000p-1f,0x1.a1d6540000000p-2f,-0x1.d36fc80000000p-1f,
0x1.9ef7940000000p-2f,-0x1.d4134e0000000p-1f,0x1.9c17d40000000p-2f,-0x1.d4b5b20000000p-1f,0x1.9937160000000p-2f,-0x1.d556f60000000p-1f,0x1.96555c0000000p-2f,-0x1.d5f7180000000p-1f,
0x1.9372a60000000p-2f,-0x1.d696180000000p-1f,0x1.908ef80000000p-2f,-0x1.d733f60000000p-1f,0x1.8daa520000000p-2f,-0x1.d7d0b00000000p-1f,0x1.8ac4b80000000p-2f,-0x1.d86c480000000p-1f,
0x1.87de2a0000000p-2f,-0x1.d906bc0000000p-1f,0x1.84f6aa0000000p-2f,-0x1.d9a00e0000000p-1f,0x1.820e3c0000000p-2f,-0x1.da383a0000000p-1f,0x1.7f24de0000000p-2f,-0x1.dacf420000000p-1f,
0x1.7c3a940000000p-2f,-0x1.db65260000000p-1f,0x1.794f5e0000000p-2f,-0x1.dbf9e40000000p-1f,0x1.7663400000000p-2f,-0x1.dc8d7c0000000p-1f,0x1.73763c0000000p-2f,-0x1.dd1ff00000000p-1f,
0x1.7088540000000p-2f,-0x1.ddb13c0000000p-1f,0x1.6d99860000000p-2f,-0x1.de41600000000p-1f,0x1.6aa9d80000000p-2f,-0x1.ded0600000000p-1f,0x1.67b94a0000000p-2f,-0x1.df5e360000000p-1f,
0x1.64c7de0000000p-2f,-0x1.dfeae60000000p-1f,0x1.61d5960000000p-2f,-0x1.e0766e0000000p-1f,0x1.5ee2740000000p-2f,-0x1.e100cc0000000p-1f,0x1.5bee780000000p-2f,-0x1.e18a020000000p-1f,
0x1.58f9a80000000p-2f,-0x1.e212100000000p-1f,0x1.5604020000000p-2f,-0x1.e298f40000000p-1f,0x1.530d880000000p-2f,-0x1.e31eae0000000p-1f,0x1.50163e0000000p-2f,-0x1.e3a33e0000000p-1f,
0x1.4d1e240000000p-2f,-0x1.e426a40000000p-1f,0x1.4a253e0000000p-2f,-0x1.e4a8e00000000p-1f,0x1.472b8a0000000p-2f,-0x1.e529f00000000p-1f,0x1.44310e0000000p-2f,-0x1.e5a9d60000000p-1f,
0x1.4135ca0000000p-2f,-0x1.e6288e0000000p-1f,0x1.3e39be0000000p-2f,-0x1.e6a61c0000000p-1f,0x1.3b3cf00000000p-2f,-0x1.e7227e0000000p-1f,0x1.383f5e0000000p-2f,-0x1.e79db20000000p-1f,
0x1.35410c0000000p-2f,-0x1.e817ba0000000p-1f,0x1.3241fc0000000p-2f,-0x1.e890960000000p-1f,0x1.2f422e0000000p-2f,-0x1.e908440000000p-1f,0x1.2c41a40000000p-2f,-0x1.e97ec40000000p-1f,
0x1.2940620000000p-2f,-0x1.e9f4160000000p-1f,0x1.263e6a0000000p-2f,-0x1.ea683a0000000p-1f,0x1.233bba0000000p-2f,-0x1.eadb2e0000000p-1f,0x1.2038580000000p-2f,-0x1.eb4cf60000000p-1f,
0x1.1d34440000000p-2f,-0x1.ebbd8c0000000p-1f,0x1.1a2f800000000p-2f,-0x1.ec2cf40000000p-1f,0x1.172a0e0000000p-2f,-0x1.ec9b2e0000000p-1f,0x1.1423ee0000000p-2f,-0x1.ed08360000000p-1f,
0x1.111d260000000p-2f,-0x1.ed740e0000000p-1f,0x1.0e15b40000000p-2f,-0x1.eddeb60000000p-1f,0x1.0b0d9c0000000p-2f,-0x1.ee482e0000000p-1f,0x1.0804e00000000p-2f,-0x1.eeb0740000000p-1f,
0x1.04fb800000000p-2f,-0x1.ef178a0000000p-1f,0x1.01f1800000000p-2f,-0x1.ef7d6e0000000p-1f,0x1.fdcdc20000000p-3f,-0x1.efe2200000000p-1f,0x1.f7b7480000000p-3f,-0x1.f045a20000000p-1f,
0x1.f19f980000000p-3f,-0x1.f0a7f00000000p-1f,0x1.eb86b40000000p-3f,-0x1.f1090c0000000p-1f,0x1.e56ca20000000p-3f,-0x1.f168f60000000p-1f,0x1.df51640000000p-3f,-0x1.f1c7ac0000000p-1f,
0x1.d934fe0000000p-3f,-0x1.f225300000000p-1f,0x1.d317740000000p-3f,-0x1.f281800000000p-1f,0x1.ccf8cc0000000p-3f,-0x1.f2dc9c0000000p-1f,0x1.c6d9060000000p-3f,-0x1.f336860000000p-1f,
0x1.c0b8260000000p-3f,-0x1.f38f3a0000000p-1f,0x1.ba96340000000p-3f,-0x1.f3e6bc0000000p-1f,0x1.b4732e0000000p-3f,-0x1.f43d080000000p-1f,0x1.ae4f1e0000000p-3f,-0x1.f492200000000p-1f,
0x1.a82a020000000p-3f,-0x1.f4e6040000000p-1f,0x1.a203e20000000p-3f,-0x1.f538b20000000p-1f,0x1.9bdcc00000000p-3f,-0x1.f58a2c0000000p-1f,0x1.95b49e0000000p-3f,-0x1.f5da6e0000000p-1f,
0x1.8f8b840000000p-3f,-0x1.f6297c0000000p-1f,0x1.8961720000000p-3f,-0x1.f677560000000p-1f,0x1.83366e0000000p-3f,-0x1.f6c3f80000000p-1f,0x1.7d0a7c0000000p-3f,-0x1.f70f640000000p-1f,
0x1.76dd9e0000000p-3f,-0x1.f7599a0000000p-1f,0x1.70afd80000000p-3f,-0x1.f7a29a0000000p-1f,0x1.6a81300000000p-3f,-0x1.f7ea620000000p-1f,0x1.6451a80000000p-3f,-0x1.f830f40000000p-1f,
0x1.5e21440000000p-3f,-0x1.f876500000000p-1f,0x1.57f0080000000p-3f,-0x1.f8ba740000000p-1f,0x1.51bdf80000000p-3f,-0x1.f8fd600000000p-1f,0x1.4b8b180000000p-3f,-0x1.f93f140000000p-1f,
0x1.45576c0000000p-3f,-0x1.f97f920000000p-1f,0x1.3f22f60000000p-3f,-0x1.f9bed80000000p-1f,0x1.38edbc0000000p-3f,-0x1.f9fce60000000p-1f,0x1.32b7c00000000p-3f,-0x1.fa39ba0000000p-1f,
0x1.2c81060000000p-3f,-0x1.fa75580000000p-1f,0x1.2649940000000p-3f,-0x1.faafbc0000000p-1f,0x1.20116e0000000p-3f,-0x1.fae8e80000000p-1f,0x1.19d8940000000p-3f,-0x1.fb20dc0000000p-1f,
0x1.139f0c0000000p-3f,-0x1.fb57980000000p-1f,0x1.0d64dc0000000p-3f,-0x1.fb8d180000000p-1f,0x1.072a040000000p-3f,-0x1.fbc1620000000p-1f,0x1.00ee8a0000000p-3f,-0x1.fbf4700000000p-1f,
0x1.f564e60000000p-4f,-0x1.fc26480000000p-1f,0x1.e8eb800000000p-4f,-0x1.fc56e40000000p-1f,0x1.dc70ec0000000p-4f,-0x1.fc86460000000p-1f,0x1.cff5340000000p-4f,-0x1.fcb4700000000p-1f,
0x1.c3785c0000000p-4f,-0x1.fce1600000000p-1f,0x1.b6fa6e0000000p-4f,-0x1.fd0d160000000p-1f,0x1.aa7b720000000p-4f,-0x1.fd37920000000p-1f,0x1.9dfb6e0000000p-4f,-0x1.fd60d20000000p-1f,
0x1.917a6c0000000p-4f,-0x1.fd88da0000000p-1f,0x1.84f8720000000p-4f,-0x1.fdafa80000000p-1f,0x1.7875860000000p-4f,-0x1.fdd53a0000000p-1f,0x1.6bf1b40000000p-4f,-0x1.fdf9920000000p-1f,
0x1.5f6d000000000p-4f,-0x1.fe1cb00000000p-1f,0x1.52e7740000000p-4f,-0x1.fe3e920000000p-1f,0x1.4661180000000p-4f,-0x1.fe5f3a0000000p-1f,0x1.39d9f20000000p-4f,-0x1.fe7ea80000000p-1f,
0x1.2d520a0000000p-4f,-0x1.fe9cda0000000p-1f,0x1.20c9680000000p-4f,-0x1.feb9d20000000p-1f,0x1.1440140000000p-4f,-0x1.fed58e0000000p-1f,0x1.07b6140000000p-4f,-0x1.fef0100000000p-1f,
0x1.f656e80000000p-5f,-0x1.ff09560000000p-1f,0x1.dd40700000000p-5f,-0x1.ff21620000000p-1f,0x1.c428d20000000p-5f,-0x1.ff38300000000p-1f,0x1.ab101c0000000p-5f,-0x1.ff4dc60000000p-1f,
0x1.91f6600000000p-5f,-0x1.ff621e0000000p-1f,0x1.78dbaa0000000p-5f,-0x1.ff753c0000000p-1f,0x1.5fc00e0000000p-5f,-0x1.ff871e0000000p-1f,0x1.46a3960000000p-5f,-0x1.ff97c40000000p-1f,
0x1.2d86580000000p-5f,-0x1.ffa72e0000000p-1f,0x1.14685e0000000p-5f,-0x1.ffb55e0000000p-1f,0x1.f693740000000p-6f,-0x1.ffc2520000000p-1f,0x1.c454f40000000p-6f,-0x1.ffce0a0000000p-1f,
0x1.9215600000000p-6f,-0x1.ffd8860000000p-1f,0x1.5fd4d20000000p-6f,-0x1.ffe1c60000000p-1f,0x1.2d936c0000000p-6f,-0x1.ffe9cc0000000p-1f,0x1.f6a2960000000p-7f,-0x1.fff0940000000p-1f,
0x1.921d200000000p-7f,-0x1.fff6220000000p-1f,0x1.2d96b00000000p-7f,-0x1.fffa720000000p-1f,0x1.921f100000000p-8f,-0x1.fffd880000000p-1f,0x1.921f8c0000000p-9f,-0x1.ffff620000000p-1f,
0.0f,-0x1.0000000000000p+0f,-0x1.921f8c0000000p-9f,-0x1.ffff620000000p-1f,-0x1.921f100000000p-8f,-0x1.fffd880000000p-1f,-0x1.2d96b00000000p-7f,-0x1.fffa720000000p-1f,
-0x1.921d200000000p-7f,-0x1.fff6220000000p-1f,-0x1.f6a2960000000p-7f,-0x1.fff0940000000p-1f,-0x1.2d936c0000000p-6f,-0x1.ffe9cc0000000p-1f,-0x1.5fd4d20000000p-6f,-0x1.ffe1c60000000p-1f,
-0x1.9215600000000p-6f,-0x1.ffd8860000000p-1f,-0x1.c454f40000000p-6f,-0x1.ffce0a0000000p-1f,-0x1.f693740000000p-6f,-0x1.ffc2520000000p-1f,-0x1.14685e0000000p-5f,-0x1.ffb55e0000000p-1f,
-0x1.2d86580000000p-5f,-0x1.ffa72e0000000p-1f,-0x1.46a3960000000p-5f,-0x1.ff97c40000000p-1f,-0x1.5fc00e0000000p-5f,-0x1.ff871e0000000p-1f,-0x1.78dbaa0000000p-5f,-0x1.ff753c0000000p-1f,
-0x1.91f6600000000p-5f,-0x1.ff621e0000000p-1f,-0x1.ab101c0000000p-5f,-0x1.ff4dc60000000p-1f,-0x1.c428d20000000p-5f,-0x1.ff38300000000p-1f,-0x1.dd40700000000p-5f,-0x1.ff21620000000p-1f,
-0x1.f656e80000000p-5f,-0x1.ff09560000000p-1f,-0x1.07b6140000000p-4f,-0x1.fef0100000000p-1f,-0x1.1440140000000p-4f,-0x1.fed58e0000000p-1f,-0x1.20c9680000000p-4f,-0x1.feb9d20000000p-1f,
-0x1.2d520a0000000p-4f,-0x1.fe9cda0000000p-1f,-0x1.39d9f20000000p-4f,-0x1.fe7ea80000000p-1f,-0x1.4661180000000p-4f,-0x1.fe5f3a0000000p-1f,-0x1.52e7740000000p-4f,-0x1.fe3e920000000p-1f,
-0x1.5f6d000000000p-4f,-0x1.fe1cb00000000p-1f,-0x1.6bf1b40000000p-4f,-0x1.fdf9920000000p-1f,-0x1.7875860000000p-4f,-0x1.fdd53a0000000p-1f,-0x1.84f8720000000p-4f,-0x1.fdafa80000000p-1f,
-0x1.917a6c0000000p-4f,-0x1.fd88da0000000p-1f,-0x1.9dfb6e0000000p-4f,-0x1.fd60d20000000p-1f,-0x1.aa7b720000000p-4f,-0x1.fd37920000000p-1f,-0x1.b6fa6e0000000p-4f,-0x1.fd0d160000000p-1f,
-0x1.c3785c0000000p-4f,-0x1.fce1600000000p-1f,-0x1.cff5340000000p-4f,-0x1.fcb4700000000p-1f,-0x1.dc70ec0000000p-4f,-0x1.fc86460000000p-1f,-0x1.e8eb800000000p-4f,-0x1.fc56e40000000p-1f,
-0x1.f564e60000000p-4f,-0x1.fc26480000000p-1f,-0x1.00ee8a0000000p-3f,-0x1.fbf4700000000p-1f,-0x1.072a040000000p-3f,-0x1.fbc1620000000p-1f,-0x1.0d64dc0000000p-3f,-0x1.fb8d180000000p-1f,
-0x1.139f0c0000000p-3f,-0x1.fb57980000000p-1f,-0x1.19d8940000000p-3f,-0x1.fb20dc0000000p-1f,-0x1.20116e0000000p-3f,-0x1.fae8e80000000p-1f,-0x1.2649940000000p-3f,-0x1.faafbc0000000p-1f,
-0x1.2c81060000000p-3f,-0x1.fa75580000000p-1f,-0x1.32b7c00000000p-3f,-0x1.fa39ba0000000p-1f,-0x1.38edbc0000000p-3f,-0x1.f9fce60000000p-1f,-0x1.3f22f60000000p-3f,-0x1.f9bed80000000p-1f,
-0x1.45576c0000000p-3f,-0x1.f97f920000000p-1f,-0x1.4b8b180000000p-3f,-0x1.f93f140000000p-1f,-0x1.51bdf80000000p-3f,-0x1.f8fd600000000p-1f,-0x1.57f0080000000p-3f,-0x1.f8ba740000000p-1f,
-0x1.5e21440000000p-3f,-0x1.f876500000000p-1f,-0x1.6451a80000000p-3f,-0x1.f830f40000000p-1f,-0x1.6a81300000000p-3f,-0x1.f7ea620000000p-1f,-0x1.70afd80000000p-3f,-0x1.f7a29a0000000p-1f,
-0x1.76dd9e0000000p-3f,-0x1.f7599a0000000p-1f,-0x1.7d0a7c0000000p-3f,-0x1.f70f640000000p-1f,-0x1.83366e0000000p-3f,-0x1.f6c3f80000000p-1f,-0x1.8961720000000p-3f,-0x1.f677560000000p-1f,
-0x1.8f8b840000000p-3f,-0x1.f6297c0000000p-1f,-0x1.95b49e0000000p-3f,-0x1.f5da6e0000000p-1f,-0x1.9bdcc00000000p-3f,-0x1.f58a2c0000000p-1f,-0x1.a203e20000000p-3f,-0x1.f538b20000000p-1f,
-0x1.a82a020000000p-3f,-0x1.f4e6040000000p-1f,-0x1.ae4f1e0000000p-3f,-0x1.f492200000000p-1f,-0x1.b4732e0000000p-3f,-0x1.f43d080000000p-1f,-0x1.ba96340000000p-3f,-0x1.f3e6bc0000000p-1f,
-0x1.c0b8260000000p-3f,-0x1.f38f3a0000000p-1f,-0x1.c6d9060000000p-3f,-0x1.f336860000000p-1f,-0x1.ccf8cc0000000p-3f,-0x1.f2dc9c0000000p-1f,-0x1.d317740000000p-3f,-0x1.f281800000000p-1f,
-0x1.d934fe0000000p-3f,-0x1.f225300000000p-1f,-0x1.df51640000000p-3f,-0x1.f1c7ac0000000p-1f,-0x1.e56ca20000000p-3f,-0x1.f168f60000000p-1f,-0x1.eb86b40000000p-3f,-0x1.f1090c0000000p-1f,
-0x1.f19f980000000p-3f,-0x1.f0a7f00000000p-1f,-0x1.f7b7480000000p-3f,-0x1.f045a20000000p-1f,-0x1.fdcdc20000000p-3f,-0x1.efe2200000000p-1f,-0x1.01f1800000000p-2f,-0x1.ef7d6e0000000p-1f,
-0x1.04fb800000000p-2f,-0x1.ef178a0000000p-1f,-0x1.0804e00000000p-2f,-0x1.eeb0740000000p-1f,-0x1.0b0d9c0000000p-2f,-0x1.ee482e0000000p-1f,-0x1.0e15b40000000p-2f,-0x1.eddeb60000000p-1f,
-0x1.111d260000000p-2f,-0x1.ed740e0000000p-1f,-0x1.1423ee0000000p-2f,-0x1.ed08360000000p-1f,-0x1.172a0e0000000p-2f,-0x1.ec9b2e0000000p-1f,-0x1.1a2f800000000p-2f,-0x1.ec2cf40000000p-1f,
-0x1.1d34440000000p-2f,-0x1.ebbd8c0000000p-1f,-0x1.2038580000000p-2f,-0x1.eb4cf60000000p-1f,-0x1.233bba0000000p-2f,-0x1.eadb2e0000000p-1f,-0x1.263e6a0000000p-2f,-0x1.ea683a0000000p-1f,
-0x1.2940620000000p-2f,-0x1.e9f4160000000p-1f,-0x1.2c41a40000000p-2f,-0x1.e97ec40000000p-1f,-0x1.2f422e0000000p-2f,-0x1.e908440000000p-1f,-0x1.3241fc0000000p-2f,-0x1.e890960000000p-1f,
-0x1.35410c0000000p-2f,-0x1.e817ba0000000p-1f,-0x1.383f5e0000000p-2f,-0x1.e79db20000000p-1f,-0x1.3b3cf00000000p-2f,-0x1.e7227e0000000p-1f,-0x1.3e39be0000000p-2f,-0x1.e6a61c0000000p-1f,
-0x1.4135ca0000000p-2f,-0x1.e6288e0000000p-1f,-0x1.44310e0000000p-2f,-0x1.e5a9d60000000p-1f,-0x1.472b8a0000000p-2f,-0x1.e529f00000000p-1f,-0x1.4a253e0000000p-2f,-0x1.e4a8e00000000p-1f,
-0x1.4d1e240000000p-2f,-0x1.e426a40000000p-1f,-0x1.50163e0000000p-2f,-0x1.e3a33e0000000p-1f,-0x1.530d880000000p-2f,-0x1.e31eae0000000p-1f,-0x1.5604020000000p-2f,-0x1.e298f40000000p-1f,
-0x1.58f9a80000000p-2f,-0x1.e212100000000p-1f,-0x1.5bee780000000p-2f,-0x1.e18a020000000p-1f,-0x1.5ee2740000000p-2f,-0x1.e100cc0000000p-1f,-0x1.61d5960000000p-2f,-0x1.e0766e0000000p-1f,
-0x1.64c7de0000000p-2f,-0x1.dfeae60000000p-1f,-0x1.67b94a0000000p-2f,-0x1.df5e360000000p-1f,-0x1.6aa9d80000000p-2f,-0x1.ded0600000000p-1f,-0x1.6d99860000000p-2f,-0x1.de41600000000p-1f,
-0x1.7088540000000p-2f,-0x1.ddb13c0000000p-1f,-0x1.73763c0000000p-2f,-0x1.dd1ff00000000p-1f,-0x1.7663400000000p-2f,-0x1.dc8d7c0000000p-1f,-0x1.794f5e0000000p-2f,-0x1.dbf9e40000000p-1f,
-0x1.7c3a940000000p-2f,-0x1.db65260000000p-1f,-0x1.7f24de0000000p-2f,-0x1.dacf420000000p-1f,-0x1.820e3c0000000p-2f,-0x1.da383a0000000p-1f,-0x1.84f6aa0000000p-2f,-0x1.d9a00e0000000p-1f,
-0x1.87de2a0000000p-2f,-0x1.d906bc0000000p-1f,-0x1.8ac4b80000000p-2f,-0x1.d86c480000000p-1f,-0x1.8daa520000000p-2f,-0x1.d7d0b00000000p-1f,-0x1.908ef80000000p-2f,-0x1.d733f60000000p-1f,
-0x1.9372a60000000p-2f,-0x1.d696180000000p-1f,-0x1.96555c0000000p-2f,-0x1.d5f7180000000p-1f,-0x1.9937160000000p-2f,-0x1.d556f60000000p-1f,-0x1.9c17d40000000p-2f,-0x1.d4b5b20000000p-1f,
-0x1.9ef7940000000p-2f,-0x1.d4134e0000000p-1f,-0x1.a1d6540000000p-2f,-0x1.d36fc80000000p-1f,-0x1.a4b4120000000p-2f,-0x1.d2cb220000000p-1f,-0x1.a790ce0000000p-2f,-0x1.d2255c0000000p-1f,
-0x1.aa6c820000000p-2f,-0x1.d17e780000000p-1f,-0x1.ad47320000000p-2f,-0x1.d0d6720000000p-1f,-0x1.b020d60000000p-2f,-0x1.d02d500000000p-1f,-0x1.b2f9720000000p-2f,-0x1.cf830e0000000p-1f,
-0x1.b5d1000000000p-2f,-0x1.ced7b00000000p-1f,-0x1.b8a7820000000p-2f,-0x1.ce2b320000000p-1f,-0x1.bb7cf20000000p-2f,-0x1.cd7d980000000p-1f,-0x1.be51520000000p-2f,-0x1.cccee20000000p-1f,
-0x1.c1249e0000000p-2f,-0x1.cc1f100000000p-1f,-0x1.c3f6d40000000p-2f,-0x1.cb6e200000000p-1f,-0x1.c6c7f40000000p-2f,-0x1.cabc160000000p-1f,-0x1.c997fc0000000p-2f,-0x1.ca08f20000000p-1f,
-0x1.cc66ea0000000p-2f,-0x1.c954b20000000p-1f,-0x1.cf34ba0000000p-2f,-0x1.c89f580000000p-1f,-0x1.d2016e0000000p-2f,-0x1.c7e8e60000000p-1f,-0x1.d4cd020000000p-2f,-0x1.c731580000000p-1f,
-0x1.d797760000000p-2f,-0x1.c678b40000000p-1f,-0x1.da60c60000000p-2f,-0x1.c5bef60000000p-1f,-0x1.dd28f20000000p-2f,-0x1.c504200000000p-1f,-0x1.dfeff60000000p-2f,-0x1.c448340000000p-1f,
-0x1.e2b5d40000000p-2f,-0x1.c38b300000000p-1f,-0x1.e57a860000000p-2f,-0x1.c2cd140000000p-1f,-0x1.e83e0e0000000p-2f,-0x1.c20de40000000p-1f,-0x1.eb006a0000000p-2f,-0x1.c14d9e0000000p-1f,
-0x1.edc1960000000p-2f,-0x1.c08c420000000p-1f,-0x1.f081900000000p-2f,-0x1.bfc9d20000000p-1f,-0x1.f3405a0000000p-2f,-0x1.bf064e0000000p-1f,-0x1.f5fdee0000000p-2f,-0x1.be41b60000000p-1f,
-0x1.f8ba4e0000000p-2f,-0x1.bd7c0a0000000p-1f,-0x1.fb75760000000p-2f,-0x1.bcb54c0000000p-1f,-0x1.fe2f640000000p-2f,-0x1.bbed7c0000000p-1f,-0x1.00740c0000000p-1f,-0x1.bb249a0000000p-1f,
-0x1.01cfc80000000p-1f,-0x1.ba5aa60000000p-1f,-0x1.032ae60000000p-1f,-0x1.b98fa20000000p-1f,-0x1.0485620000000p-1f,-0x1.b8c38e0000000p-1f,-0x1.05df3e0000000p-1f,-0x1.b7f6680000000p-1f,
-0x1.07387a0000000p-1f,-0x1.b728340000000p-1f,-0x1.0891120000000p-1f,-0x1.b658f20000000p-1f,-0x1.09e9080000000p-1f,-0x1.b588a00000000p-1f,-0x1.0b40580000000p-1f,-0x1.b4b7400000000p-1f,
-0x1.0c97040000000p-1f,-0x1.b3e4d40000000p-1f,-0x1.0ded0c0000000p-1f,-0x1.b3115a0000000p-1f,-0x1.0f426c0000000p-1f,-0x1.b23cd40000000p-1f,-0x1.1097240000000p-1f,-0x1.b167420000000p-1f,
-0x1.11eb360000000p-1f,-0x1.b090a60000000p-1f,-0x1.133e9c0000000p-1f,-0x1.afb8fe0000000p-1f,-0x1.14915a0000000p-1f,-0x1.aee04c0000000p-1f,-0x1.15e36e0000000p-1f,-0x1.ae06900000000p-1f,
-0x1.1734d60000000p-1f,-0x1.ad2bca0000000p-1f,-0x1.1885920000000p-1f,-0x1.ac4ffc0000000p-1f,-0x1.19d5a00000000p-1f,-0x1.ab73260000000p-1f,-0x1.1b25020000000p-1f,-0x1.aa95480000000p-1f,
-0x1.1c73b40000000p-1f,-0x1.a9b6620000000p-1f,-0x1.1dc1b60000000p-1f,-0x1.a8d6760000000p-1f,-0x1.1f0f080000000p-1f,-0x1.a7f5860000000p-1f,-0x1.205baa0000000p-1f,-0x1.a7138e0000000p-1f,
-0x1.21a79a0000000p-1f,-0x1.a630920000000p-1f,-0x1.22f2d60000000p-1f,-0x1.a54c920000000p-1f,-0x1.243d600000000p-1f,-0x1.a4678c0000000p-1f,-0x1.2587340000000p-1f,-0x1.a381840000000p-1f,
-0x1.26d0540000000p-1f,-0x1.a29a7a0000000p-1f,-0x1.2818be0000000p-1f,-0x1.a1b26e0000000p-1f,-0x1.2960720000000p-1f,-0x1.a0c95e0000000p-1f,-0x1.2aa76e0000000p-1f,-0x1.9fdf500000000p-1f,
-0x1.2bedb20000000p-1f,-0x1.9ef43e0000000p-1f,-0x1.2d333e0000000p-1f,-0x1.9e082e0000000p-1f,-0x1.2e780e0000000p-1f,-0x1.9d1b200000000p-1f,-0x1.2fbc240000000p-1f,-0x1.9c2d120000000p-1f,
-0x1.30ff800000000p-1f,-0x1.9b3e040000000p-1f,-0x1.32421e0000000p-1f,-0x1.9a4dfa0000000p-1f,-0x1.3384000000000p-1f,-0x1.995cf20000000p-1f,-0x1.34c5260000000p-1f,-0x1.986af00000000p-1f,
-0x1.36058c0000000p-1f,-0x1.9777f00000000p-1f,-0x1.3745320000000p-1f,-0x1.9683f40000000p-1f,-0x1.3884180000000p-1f,-0x1.958efe0000000p-1f,-0x1.39c23e0000000p-1f,-0x1.94990e0000000p-1f,
-0x1.3affa20000000p-1f,-0x1.93a2240000000p-1f,-0x1.3c3c440000000p-1f,-0x1.92aa420000000p-1f,-0x1.3d78240000000p-1f,-0x1.91b1660000000p-1f,-0x1.3eb33e0000000p-1f,-0x1.90b7940000000p-1f,
-0x1.3fed960000000p-1f,-0x1.8fbcca0000000p-1f,-0x1.4127260000000p-1f,-0x1.8ec10a0000000p-1f,-0x1.425ff20000000p-1f,-0x1.8dc4540000000p-1f,-0x1.4397f60000000p-1f,-0x1.8cc6a80000000p-1f,
-0x1.44cf320000000p-1f,-0x1.8bc8060000000p-1f,-0x1.4605a60000000p-1f,-0x1.8ac8720000000p-1f,-0x1.473b520000000p-1f,-0x1.89c7ea0000000p-1f,-0x1.4870340000000p-1f,-0x1.88c66e0000000p-1f,
-0x1.49a44a0000000p-1f,-0x1.87c4000000000p-1f,-0x1.4ad7960000000p-1f,-0x1.86c0a20000000p-1f,-0x1.4c0a140000000p-1f,-0x1.85bc520000000p-1f,-0x1.4d3bc60000000p-1f,-0x1.84b7120000000p-1f,
-0x1.4e6cac0000000p-1f,-0x1.83b0e00000000p-1f,-0x1.4f9cc20000000p-1f,-0x1.82a9c20000000p-1f,-0x1.50cc0a0000000p-1f,-0x1.81a1b40000000p-1f,-0x1.51fa820000000p-1f,-0x1.8098b80000000p-1f,
-0x1.53282a0000000p-1f,-0x1.7f8ece0000000p-1f,-0x1.5455000000000p-1f,-0x1.7e83f80000000p-1f,-0x1.5581040000000p-1f,-0x1.7d78360000000p-1f,-0x1.56ac360000000p-1f,-0x1.7c6b8a0000000p-1f,
-0x1.57d6940000000p-1f,-0x1.7b5df20000000p-1f,-0x1.59001e0000000p-1f,-0x1.7a4f700000000p-1f,-0x1.5a28d20000000p-1f,-0x1.7940060000000p-1f,-0x1.5b50b20000000p-1f,-0x1.782fb20000000p-1f,
-0x1.5c77bc0000000p-1f,-0x1.771e760000000p-1f,-0x1.5d9dee0000000p-1f,-0x1.760c520000000p-1f,-0x1.5ec34a0000000p-1f,-0x1.74f9480000000p-1f,-0x1.5fe7cc0000000p-1f,-0x1.73e5580000000p-1f,
-0x1.610b760000000p-1f,-0x1.72d0840000000p-1f,-0x1.622e440000000p-1f,-0x1.71baca0000000p-1f,-0x1.63503a0000000p-1f,-0x1.70a42c0000000p-1f,-0x1.6471540000000p-1f,-0x1.6f8caa0000000p-1f,
-0x1.6591920000000p-1f,-0x1.6e74460000000p-1f,-0x1.66b0f40000000p-1f,-0x1.6d5afe0000000p-1f,-0x1.67cf780000000p-1f,-0x1.6c40d80000000p-1f,-0x1.68ed1e0000000p-1f,-0x1.6b25ce0000000p-1f,
-0x1.6a09e60000000p-1f,-0x1.6a09e60000000p-1f,-0x1.6b25ce0000000p-1f,-0x1.68ed1e0000000p-1f,-0x1.6c40d80000000p-1f,-0x1.67cf780000000p-1f,-0x1.6d5afe0000000p-1f,-0x1.66b0f40000000p-1f,
-0x1.6e74460000000p-1f,-0x1.6591920000000p-1f,-0x1.6f8caa0000000p-1f,-0x1.6471540000000p-1f,-0x1.70a42c0000000p-1f,-0x1.63503a0000000p-1f,-0x1.71baca0000000p-1f,-0x1.622e440000000p-1f,
-0x1.72d0840000000p-1f,-0x1.610b760000000p-1f,-0x1.73e5580000000p-1f,-0x1.5fe7cc0000000p-1f,-0x1.74f9480000000p-1f,-0x1.5ec34a0000000p-1f,-0x1.760c520000000p-1f,-0x1.5d9dee0000000p-1f,
-0x1.771e760000000p-1f,-0x1.5c77bc0000000p-1f,-0x1.782fb20000000p-1f,-0x1.5b50b20000000p-1f,-0x1.7940060000000p-1f,-0x1.5a28d20000000p-1f,-0x1.7a4f700000000p-1f,-0x1.59001e0000000p-1f,
-0x1.7b5df20000000p-1f,-0x1.57d6940000000p-1f,-0x1.7c6b8a0000000p-1f,-0x1.56ac360000000p-1f,-0x1.7d78360000000p-1f,-0x1.5581040000000p-1f,-0x1.7e83f80000000p-1f,-0x1.5455000000000p-1f,
-0x1.7f8ece0000000p-1f,-0x1.53282a0000000p-1f,-0x1.8098b80000000p-1f,-0x1.51fa820000000p-1f,-0x1.81a1b40000000p-1f,-0x1.50cc0a0000000p-1f,-0x1.82a9c20000000p-1f,-0x1.4f9cc20000000p-1f,
-0x1.83b0e00000000p-1f,-0x1.4e6cac0000000p-1f,-0x1.84b7120000000p-1f,-0x1.4d3bc60000000p-1f,-0x1.85bc520000000p-1f,-0x1.4c0a140000000p-1f,-0x1.86c0a20000000p-1f,-0x1.4ad7960000000p-1f,
-0x1.87c4000000000p-1f,-0x1.49a44a0000000p-1f,-0x1.88c66e0000000p-1f,-0x1.4870340000000p-1f,-0x1.89c7ea0000000p-1f,-0x1.473b520000000p-1f,-0x1.8ac8720000000p-1f,-0x1.4605a60000000p-1f,
-0x1.8bc8060000000p-1f,-0x1.44cf320000000p-1f,-0x1.8cc6a80000000p-1f,-0x1.4397f60000000p-1f,-0x1.8dc4540000000p-1f,-0x1.425ff20000000p-1f,-0x1.8ec10a0000000p-1f,-0x1.4127260000000p-1f,
-0x1.8fbcca0000000p-1f,-0x1.3fed960000000p-1f,-0x1.90b7940000000p-1f,-0x1.3eb33e0000000p-1f,-0x1.91b1660000000p-1f,-0x1.3d78240000000p-1f,-0x1.92aa420000000p-1f,-0x1.3c3c440000000p-1f,
-0x1.93a2240000000p-1f,-0x1.3affa20000000p-1f,-0x1.94990e0000000p-1f,-0x1.39c23e0000000p-1f,-0x1.958efe0000000p-1f,-0x1.3884180000000p-1f,-0x1.9683f40000000p-1f,-0x1.3745320000000p-1f,
-0x1.9777f00000000p-1f,-0x1.36058c0000000p-1f,-0x1.986af00000000p-1f,-0x1.34c5260000000p-1f,-0x1.995cf20000000p-1f,-0x1.3384000000000p-1f,-0x1.9a4dfa0000000p-1f,-0x1.32421e0000000p-1f,
-0x1.9b3e040000000p-1f,-0x1.30ff800000000p-1f,-0x1.9c2d120000000p-1f,-0x1.2fbc240000000p-1f,-0x1.9d1b200000000p-1f,-0x1.2e780e0000000p-1f,-0x1.9e082e0000000p-1f,-0x1.2d333e0000000p-1f,
-0x1.9ef43e0000000p-1f,-0x1.2bedb20000000p-1f,-0x1.9fdf500000000p-1f,-0x1.2aa76e0000000p-1f,-0x1.a0c95e0000000p-1f,-0x1.2960720000000p-1f,-0x1.a1b26e0000000p-1f,-0x1.2818be0000000p-1f,
-0x1.a29a7a0000000p-1f,-0x1.26d0540000000p-1f,-0x1.a381840000000p-1f,-0x1.2587340000000p-1f,-0x1.a4678c0000000p-1f,-0x1.243d600000000p-1f,-0x1.a54c920000000p-1f,-0x1.22f2d60000000p-1f,
-0x1.a630920000000p-1f,-0x1.21a79a0000000p-1f,-0x1.a7138e0000000p-1f,-0x1.205baa0000000p-1f,-0x1.a7f5860000000p-1f,-0x1.1f0f080000000p-1f,-0x1.a8d6760000000p-1f,-0x1.1dc1b60000000p-1f,
-0x1.a9b6620000000p-1f,-0x1.1c73b40000000p-1f,-0x1.aa95480000000p-1f,-0x1.1b25020000000p-1f,-0x1.ab73260000000p-1f,-0x1.19d5a00000000p-1f,-0x1.ac4ffc0000000p-1f,-0x1.1885920000000p-1f,
-0x1.ad2bca0000000p-1f,-0x1.1734d60000000p-1f,-0x1.ae06900000000p-1f,-0x1.15e36e0000000p-1f,-0x1.aee04c0000000p-1f,-0x1.14915a0000000p-1f,-0x1.afb8fe0000000p-1f,-0x1.133e9c0000000p-1f,
-0x1.b090a60000000p-1f,-0x1.11eb360000000p-1f,-0x1.b167420000000p-1f,-0x1.1097240000000p-1f,-0x1.b23cd40000000p-1f,-0x1.0f426c0000000p-1f,-0x1.b3115a0000000p-1f,-0x1.0ded0c0000000p-1f,
-0x1.b3e4d40000000p-1f,-0x1.0c97040000000p-1f,-0x1.b4b7400000000p-1f,-0x1.0b40580000000p-1f,-0x1.b588a00000000p-1f,-0x1.09e9080000000p-1f,-0x1.b658f20000000p-1f,-0x1.0891120000000p-1f,
-0x1.b728340000000p-1f,-0x1.07387a0000000p-1f,-0x1.b7f6680000000p-1f,-0x1.05df3e0000000p-1f,-0x1.b8c38e0000000p-1f,-0x1.0485620000000p-1f,-0x1.b98fa20000000p-1f,-0x1.032ae60000000p-1f,
-0x1.ba5aa60000000p-1f,-0x1.01cfc80000000p-1f,-0x1.bb249a0000000p-1f,-0x1.00740c0000000p-1f,-0x1.bbed7c0000000p-1f,-0x1.fe2f640000000p-2f,-0x1.bcb54c0000000p-1f,-0x1.fb75760000000p-2f,
-0x1.bd7c0a0000000p-1f,-0x1.f8ba4e0000000p-2f,-0x1.be41b60000000p-1f,-0x1.f5fdee0000000p-2f,-0x1.bf064e0000000p-1f,-0x1.f3405a0000000p-2f,-0x1.bfc9d20000000p-1f,-0x1.f081900000000p-2f,
-0x1.c08c420000000p-1f,-0x1.edc1960000000p-2f,-0x1.c14d9e0000000p-1f,-0x1.eb006a0000000p-2f,-0x1.c20de40000000p-1f,-0x1.e83e0e0000000p-2f,-0x1.c2cd140000000p-1f,-0x1.e57a860000000p-2f,
-0x1.c38b300000000p-1f,-0x1.e2b5d40000000p-2f,-0x1.c448340000000p-1f,-0x1.dfeff60000000p-2f,-0x1.c504200000000p-1f,-0x1.dd28f20000000p-2f,-0x1.c5bef60000000p-1f,-0x1.da60c60000000p-2f,
-0x1.c678b40000000p-1f,-0x1.d797760000000p-2f,-0x1.c731580000000p-1f,-0x1.d4cd020000000p-2f,-0x1.c7e8e60000000p-1f,-0x1.d2016e0000000p-2f,-0x1.c89f580000000p-1f,-0x1.cf34ba0000000p-2f,
-0x1.c954b20000000p-1f,-0x1.cc66ea0000000p-2f,-0x1.ca08f20000000p-1f,-0x1.c997fc0000000p-2f,-0x1.cabc160000000p-1f,-0x1.c6c7f40000000p-2f,-0x1.cb6e200000000p-1f,-0x1.c3f6d40000000p-2f,
-0x1.cc1f100000000p-1f,-0x1.c1249e0000000p-2f,-0x1.cccee20000000p-1f,-0x1.be51520000000p-2f,-0x1.cd7d980000000p-1f,-0x1.bb7cf20000000p-2f,-0x1.ce2b320000000p-1f,-0x1.b8a7820000000p-2f,
-0x1.ced7b00000000p-1f,-0x1.b5d1000000000p-2f,-0x1.cf830e0000000p-1f,-0x1.b2f9720000000p-2f,-0x1.d02d500000000p-1f,-0x1.b020d60000000p-2f,-0x1.d0d6720000000p-1f,-0x1.ad47320000000p-2f,
-0x1.d17e780000000p-1f,-0x1.aa6c820000000p-2f,-0x1.d2255c0000000p-1f,-0x1.a790ce0000000p-2f,-0x1.d2cb220000000p-1f,-0x1.a4b4120000000p-2f,-0x1.d36fc80000000p-1f,-0x1.a1d6540000000p-2f,
-0x1.d4134e0000000p-1f,-0x1.9ef7940000000p-2f,-0x1.d4b5b20000000p-1f,-0x1.9c17d40000000p-2f,-0x1.d556f60000000p-1f,-0x1.9937160000000p-2f,-0x1.d5f7180000000p-1f,-0x1.96555c0000000p-2f,
-0x1.d696180000000p-1f,-0x1.9372a60000000p-2f,-0x1.d733f60000000p-1f,-0x1.908ef80000000p-2f,-0x1.d7d0b00000000p-1f,-0x1.8daa520000000p-2f,-0x1.d86c480000000p-1f,-0x1.8ac4b80000000p-2f,
-0x1.d906bc0000000p-1f,-0x1.87de2a0000000p-2f,-0x1.d9a00e0000000p-1f,-0x1.84f6aa0000000p-2f,-0x1.da383a0000000p-1f,-0x1.820e3c0000000p-2f,-0x1.dacf420000000p-1f,-0x1.7f24de0000000p-2f,
-0x1.db65260000000p-1f,-0x1.7c3a940000000p-2f,-0x1.dbf9e40000000p-1f,-0x1.794f5e0000000p-2f,-0x1.dc8d7c0000000p-1f,-0x1.7663400000000p-2f,-0x1.dd1ff00000000p-1f,-0x1.73763c0000000p-2f,
-0x1.ddb13c0000000p-1f,-0x1.7088540000000p-2f,-0x1.de41600000000p-1f,-0x1.6d99860000000p-2f,-0x1.ded0600000000p-1f,-0x1.6aa9d80000000p-2f,-0x1.df5e360000000p-1f,-0x1.67b94a0000000p-2f,
-0x1.dfeae60000000p-1f,-0x1.64c7de0000000p-2f,-0x1.e0766e0000000p-1f,-0x1.61d5960000000p-2f,-0x1.e100cc0000000p-1f,-0x1.5ee2740000000p-2f,-0x1.e18a020000000p-1f,-0x1.5bee780000000p-2f,
-0x1.e212100000000p-1f,-0x1.58f9a80000000p-2f,-0x1.e298f40000000p-1f,-0x1.5604020000000p-2f,-0x1.e31eae0000000p-1f,-0x1.530d880000000p-2f,-0x1.e3a33e0000000p-1f,-0x1.50163e0000000p-2f,
-0x1.e426a40000000p-1f,-0x1.4d1e240000000p-2f,-0x1.e4a8e00000000p-1f,-0x1.4a253e0000000p-2f,-0x1.e529f00000000p-1f,-0x1.472b8a0000000p-2f,-0x1.e5a9d60000000p-1f,-0x1.44310e0000000p-2f,
-0x1.e6288e0000000p-1f,-0x1.4135ca0000000p-2f,-0x1.e6a61c0000000p-1f,-0x1.3e39be0000000p-2f,-0x1.e7227e0000000p-1f,-0x1.3b3cf00000000p-2f,-0x1.e79db20000000p-1f,-0x1.383f5e0000000p-2f,
-0x1.e817ba0000000p-1f,-0x1.35410c0000000p-2f,-0x1.e890960000000p-1f,-0x1.3241fc0000000p-2f,-0x1.e908440000000p-1f,-0x1.2f422e0000000p-2f,-0x1.e97ec40000000p-1f,-0x1.2c41a40000000p-2f,
-0x1.e9f4160000000p-1f,-0x1.2940620000000p-2f,-0x1.ea683a0000000p-1f,-0x1.263e6a0000000p-2f,-0x1.eadb2e0000000p-1f,-0x1.233bba0000000p-2f,-0x1.eb4cf60000000p-1f,-0x1.2038580000000p-2f,
-0x1.ebbd8c0000000p-1f,-0x1.1d34440000000p-2f,-0x1.ec2cf40000000p-1f,-0x1.1a2f800000000p-2f,-0x1.ec9b2e0000000p-1f,-0x1.172a0e0000000p-2f,-0x1.ed08360000000p-1f,-0x1.1423ee0000000p-2f,
-0x1.ed740e0000000p-1f,-0x1.111d260000000p-2f,-0x1.eddeb60000000p-1f,-0x1.0e15b40000000p-2f,-0x1.ee482e0000000p-1f,-0x1.0b0d9c0000000p-2f,-0x1.eeb0740000000p-1f,-0x1.0804e00000000p-2f,
-0x1.ef178a0000000p-1f,-0x1.04fb800000000p-2f,-0x1.ef7d6e0000000p-1f,-0x1.01f1800000000p-2f,-0x1.efe2200000000p-1f,-0x1.fdcdc20000000p-3f,-0x1.f045a20000000p-1f,-0x1.f7b7480000000p-3f,
-0x1.f0a7f00000000p-1f,-0x1.f19f980000000p-3f,-0x1.f1090c0000000p-1f,-0x1.eb86b40000000p-3f,-0x1.f168f60000000p-1f,-0x1.e56ca20000000p-3f,-0x1.f1c7ac0000000p-1f,-0x1.df51640000000p-3f,
-0x1.f225300000000p-1f,-0x1.d934fe0000000p-3f,-0x1.f281800000000p-1f,-0x1.d317740000000p-3f,-0x1.f2dc9c0000000p-1f,-0x1.ccf8cc0000000p-3f,-0x1.f336860000000p-1f,-0x1.c6d9060000000p-3f,
-0x1.f38f3a0000000p-1f,-0x1.c0b8260000000p-3f,-0x1.f3e6bc0000000p-1f,-0x1.ba96340000000p-3f,-0x1.f43d080000000p-1f,-0x1.b4732e0000000p-3f,-0x1.f492200000000p-1f,-0x1.ae4f1e0000000p-3f,
-0x1.f4e6040000000p-1f,-0x1.a82a020000000p-3f,-0x1.f538b20000000p-1f,-0x1.a203e20000000p-3f,-0x1.f58a2c0000000p-1f,-0x1.9bdcc00000000p-3f,-0x1.f5da6e0000000p-1f,-0x1.95b49e0000000p-3f,
-0x1.f6297c0000000p-1f,-0x1.8f8b840000000p-3f,-0x1.f677560000000p-1f,-0x1.8961720000000p-3f,-0x1.f6c3f80000000p-1f,-0x1.83366e0000000p-3f,-0x1.f70f640000000p-1f,-0x1.7d0a7c0000000p-3f,
-0x1.f7599a0000000p-1f,-0x1.76dd9e0000000p-3f,-0x1.f7a29a0000000p-1f,-0x1.70afd80000000p-3f,-0x1.f7ea620000000p-1f,-0x1.6a81300000000p-3f,-0x1.f830f40000000p-1f,-0x1.6451a80000000p-3f,
-0x1.f876500000000p-1f,-0x1.5e21440000000p-3f,-0x1.f8ba740000000p-1f,-0x1.57f0080000000p-3f,-0x1.f8fd600000000p-1f,-0x1.51bdf80000000p-3f,-0x1.f93f140000000p-1f,-0x1.4b8b180000000p-3f,
-0x1.f97f920000000p-1f,-0x1.45576c0000000p-3f,-0x1.f9bed80000000p-1f,-0x1.3f22f60000000p-3f,-0x1.f9fce60000000p-1f,-0x1.38edbc0000000p-3f,-0x1.fa39ba0000000p-1f,-0x1.32b7c00000000p-3f,
-0x1.fa75580000000p-1f,-0x1.2c81060000000p-3f,-0x1.faafbc0000000p-1f,-0x1.2649940000000p-3f,-0x1.fae8e80000000p-1f,-0x1.20116e0000000p-3f,-0x1.fb20dc0000000p-1f,-0x1.19d8940000000p-3f,
-0x1.fb57980000000p-1f,-0x1.139f0c0000000p-3f,-0x1.fb8d180000000p-1f,-0x1.0d64dc0000000p-3f,-0x1.fbc1620000000p-1f,-0x1.072a040000000p-3f,-0x1.fbf4700000000p-1f,-0x1.00ee8a0000000p-3f,
-0x1.fc26480000000p-1f,-0x1.f564e60000000p-4f,-0x1.fc56e40000000p-1f,-0x1.e8eb800000000p-4f,-0x1.fc86460000000p-1f,-0x1.dc70ec0000000p-4f,-0x1.fcb4700000000p-1f,-0x1.cff5340000000p-4f,
-0x1.fce1600000000p-1f,-0x1.c3785c0000000p-4f,-0x1.fd0d160000000p-1f,-0x1.b6fa6e0000000p-4f,-0x1.fd37920000000p-1f,-0x1.aa7b720000000p-4f,-0x1.fd60d20000000p-1f,-0x1.9dfb6e0000000p-4f,
-0x1.fd88da0000000p-1f,-0x1.917a6c0000000p-4f,-0x1.fdafa80000000p-1f,-0x1.84f8720000000p-4f,-0x1.fdd53a0000000p-1f,-0x1.7875860000000p-4f,-0x1.fdf9920000000p-1f,-0x1.6bf1b40000000p-4f,
-0x1.fe1cb00000000p-1f,-0x1.5f6d000000000p-4f,-0x1.fe3e920000000p-1f,-0x1.52e7740000000p-4f,-0x1.fe5f3a0000000p-1f,-0x1.4661180000000p-4f,-0x1.fe7ea80000000p-1f,-0x1.39d9f20000000p-4f,
-0x1.fe9cda0000000p-1f,-0x1.2d520a0000000p-4f,-0x1.feb9d20000000p-1f,-0x1.20c9680000000p-4f,-0x1.fed58e0000000p-1f,-0x1.1440140000000p-4f,-0x1.fef0100000000p-1f,-0x1.07b6140000000p-4f,
-0x1.ff09560000000p-1f,-0x1.f656e80000000p-5f,-0x1.ff21620000000p-1f,-0x1.dd40700000000p-5f,-0x1.ff38300000000p-1f,-0x1.c428d20000000p-5f,-0x1.ff4dc60000000p-1f,-0x1.ab101c0000000p-5f,
-0x1.ff621e0000000p-1f,-0x1.91f6600000000p-5f,-0x1.ff753c0000000p-1f,-0x1.78dbaa0000000p-5f,-0x1.ff871e0000000p-1f,-0x1.5fc00e0000000p-5f,-0x1.ff97c40000000p-1f,-0x1.46a3960000000p-5f,
-0x1.ffa72e0000000p-1f,-0x1.2d86580000000p-5f,-0x1.ffb55e0000000p-1f,-0x1.14685e0000000p-5f,-0x1.ffc2520000000p-1f,-0x1.f693740000000p-6f,-0x1.ffce0a0000000p-1f,-0x1.c454f40000000p-6f,
-0x1.ffd8860000000p-1f,-0x1.9215600000000p-6f,-0x1.ffe1c60000000p-1f,-0x1.5fd4d20000000p-6f,-0x1.ffe9cc0000000p-1f,-0x1.2d936c0000000p-6f,-0x1.fff0940000000p-1f,-0x1.f6a2960000000p-7f,
-0x1.fff6220000000p-1f,-0x1.921d200000000p-7f,-0x1.fffa720000000p-1f,-0x1.2d96b00000000p-7f,-0x1.fffd880000000p-1f,-0x1.921f100000000p-8f,-0x1.ffff620000000p-1f,-0x1.921f8c0000000p-9f,
-0x1.0000000000000p+0f,0.0f,-0x1.ffff620000000p-1f,0x1.921f8c0000000p-9f,-0x1.fffd880000000p-1f,0x1.921f100000000p-8f,-0x1.fffa720000000p-1f,0x1.2d96b00000000p-7f,
-0x1.fff6220000000p-1f,0x1.921d200000000p-7f,-0x1.fff0940000000p-1f,0x1.f6a2960000000p-7f,-0x1.ffe9cc0000000p-1f,0x1.2d936c0000000p-6f,-0x1.ffe1c60000000p-1f,0x1.5fd4d20000000p-6f,
-0x1.ffd8860000000p-1f,0x1.9215600000000p-6f,-0x1.ffce0a0000000p-1f,0x1.c454f40000000p-6f,-0x1.ffc2520000000p-1f,0x1.f693740000000p-6f,-0x1.ffb55e0000000p-1f,0x1.14685e0000000p-5f,
-0x1.ffa72e0000000p-1f,0x1.2d86580000000p-5f,-0x1.ff97c40000000p-1f,0x1.46a3960000000p-5f,-0x1.ff871e0000000p-1f,0x1.5fc00e0000000p-5f,-0x1.ff753c0000000p-1f,0x1.78dbaa0000000p-5f,
-0x1.ff621e0000000p-1f,0x1.91f6600000000p-5f,-0x1.ff4dc60000000p-1f,0x1.ab101c0000000p-5f,-0x1.ff38300000000p-1f,0x1.c428d20000000p-5f,-0x1.ff21620000000p-1f,0x1.dd40700000000p-5f,
-0x1.ff09560000000p-1f,0x1.f656e80000000p-5f,-0x1.fef0100000000p-1f,0x1.07b6140000000p-4f,-0x1.fed58e0000000p-1f,0x1.1440140000000p-4f,-0x1.feb9d20000000p-1f,0x1.20c9680000000p-4f,
-0x1.fe9cda0000000p-1f,0x1.2d520a0000000p-4f,-0x1.fe7ea80000000p-1f,0x1.39d9f20000000p-4f,-0x1.fe5f3a0000000p-1f,0x1.4661180000000p-4f,-0x1.fe3e920000000p-1f,0x1.52e7740000000p-4f,
-0x1.fe1cb00000000p-1f,0x1.5f6d000000000p-4f,-0x1.fdf9920000000p-1f,0x1.6bf1b40000000p-4f,-0x1.fdd53a0000000p-1f,0x1.7875860000000p-4f,-0x1.fdafa80000000p-1f,0x1.84f8720000000p-4f,
-0x1.fd88da0000000p-1f,0x1.917a6c0000000p-4f,-0x1.fd60d20000000p-1f,0x1.9dfb6e0000000p-4f,-0x1.fd37920000000p-1f,0x1.aa7b720000000p-4f,-0x1.fd0d160000000p-1f,0x1.b6fa6e0000000p-4f,
-0x1.fce1600000000p-1f,0x1.c3785c0000000p-4f,-0x1.fcb4700000000p-1f,0x1.cff5340000000p-4f,-0x1.fc86460000000p-1f,0x1.dc70ec0000000p-4f,-0x1.fc56e40000000p-1f,0x1.e8eb800000000p-4f,
-0x1.fc26480000000p-1f,0x1.f564e60000000p-4f,-0x1.fbf4700000000p-1f,0x1.00ee8a0000000p-3f,-0x1.fbc1620000000p-1f,0x1.072a040000000p-3f,-0x1.fb8d180000000p-1f,0x1.0d64dc0000000p-3f,
-0x1.fb57980000000p-1f,0x1.139f0c0000000p-3f,-0x1.fb20dc0000000p-1f,0x1.19d8940000000p-3f,-0x1.fae8e80000000p-1f,0x1.20116e0000000p-3f,-0x1.faafbc0000000p-1f,0x1.2649940000000p-3f,
-0x1.fa75580000000p-1f,0x1.2c81060000000p-3f,-0x1.fa39ba0000000p-1f,0x1.32b7c00000000p-3f,-0x1.f9fce60000000p-1f,0x1.38edbc0000000p-3f,-0x1.f9bed80000000p-1f,0x1.3f22f60000000p-3f,
-0x1.f97f920000000p-1f,0x1.45576c0000000p-3f,-0x1.f93f140000000p-1f,0x1.4b8b180000000p-3f,-0x1.f8fd600000000p-1f,0x1.51bdf80000000p-3f,-0x1.f8ba740000000p-1f,0x1.57f0080000000p-3f,
-0x1.f876500000000p-1f,0x1.5e21440000000p-3f,-0x1.f830f40000000p-1f,0x1.6451a80000000p-3f,-0x1.f7ea620000000p-1f,0x1.6a81300000000p-3f,-0x1.f7a29a0000000p-1f,0x1.70afd80000000p-3f,
-0x1.f7599a0000000p-1f,0x1.76dd9e0000000p-3f,-0x1.f70f640000000p-1f,0x1.7d0a7c0000000p-3f,-0x1.f6c3f80000000p-1f,0x1.83366e0000000p-3f,-0x1.f677560000000p-1f,0x1.8961720000000p-3f,
-0x1.f6297c0000000p-1f,0x1.8f8b840000000p-3f,-0x1.f5da6e0000000p-1f,0x1.95b49e0000000p-3f,-0x1.f58a2c0000000p-1f,0x1.9bdcc00000000p-3f,-0x1.f538b20000000p-1f,0x1.a203e20000000p-3f,
-0x1.f4e6040000000p-1f,0x1.a82a020000000p-3f,-0x1.f492200000000p-1f,0x1.ae4f1e0000000p-3f,-0x1.f43d080000000p-1f,0x1.b4732e0000000p-3f,-0x1.f3e6bc0000000p-1f,0x1.ba96340000000p-3f,
-0x1.f38f3a0000000p-1f,0x1.c0b8260000000p-3f,-0x1.f336860000000p-1f,0x1.c6d9060000000p-3f,-0x1.f2dc9c0000000p-1f,0x1.ccf8cc0000000p-3f,-0x1.f281800000000p-1f,0x1.d317740000000p-3f,
-0x1.f225300000000p-1f,0x1.d934fe0000000p-3f,-0x1.f1c7ac0000000p-1f,0x1.df51640000000p-3f,-0x1.f168f60000000p-1f,0x1.e56ca20000000p-3f,-0x1.f1090c0000000p-1f,0x1.eb86b40000000p-3f,
-0x1.f0a7f00000000p-1f,0x1.f19f980000000p-3f,-0x1.f045a20000000p-1f,0x1.f7b7480000000p-3f,-0x1.efe2200000000p-1f,0x1.fdcdc20000000p-3f,-0x1.ef7d6e0000000p-1f,0x1.01f1800000000p-2f,
-0x1.ef178a0000000p-1f,0x1.04fb800000000p-2f,-0x1.eeb0740000000p-1f,0x1.0804e00000000p-2f,-0x1.ee482e0000000p-1f,0x1.0b0d9c0000000p-2f,-0x1.eddeb60000000p-1f,0x1.0e15b40000000p-2f,
-0x1.ed740e0000000p-1f,0x1.111d260000000p-2f,-0x1.ed08360000000p-1f,0x1.1423ee0000000p-2f,-0x1.ec9b2e0000000p-1f,0x1.172a0e0000000p-2f,-0x1.ec2cf40000000p-1f,0x1.1a2f800000000p-2f,
-0x1.ebbd8c0000000p-1f,0x1.1d34440000000p-2f,-0x1.eb4cf60000000p-1f,0x1.2038580000000p-2f,-0x1.eadb2e0000000p-1f,0x1.233bba0000000p-2f,-0x1.ea683a0000000p-1f,0x1.263e6a0000000p-2f,
-0x1.e9f4160000000p-1f,0x1.2940620000000p-2f,-0x1.e97ec40000000p-1f,0x1.2c41a40000000p-2f,-0x1.e908440000000p-1f,0x1.2f422e0000000p-2f,-0x1.e890960000000p-1f,0x1.3241fc0000000p-2f,
-0x1.e817ba0000000p-1f,0x1.35410c0000000p-2f,-0x1.e79db20000000p-1f,0x1.383f5e0000000p-2f,-0x1.e7227e0000000p-1f,0x1.3b3cf00000000p-2f,-0x1.e6a61c0000000p-1f,0x1.3e39be0000000p-2f,
-0x1.e6288e0000000p-1f,0x1.4135ca0000000p-2f,-0x1.e5a9d60000000p-1f,0x1.44310e0000000p-2f,-0x1.e529f00000000p-1f,0x1.472b8a0000000p-2f,-0x1.e4a8e00000000p-1f,0x1.4a253e0000000p-2f,
-0x1.e426a40000000p-1f,0x1.4d1e240000000p-2f,-0x1.e3a33e0000000p-1f,0x1.50163e0000000p-2f,-0x1.e31eae0000000p-1f,0x1.530d880000000p-2f,-0x1.e298f40000000p-1f,0x1.5604020000000p-2f,
-0x1.e212100000000p-1f,0x1.58f9a80000000p-2f,-0x1.e18a020000000p-1f,0x1.5bee780000000p-2f,-0x1.e100cc0000000p-1f,0x1.5ee2740000000p-2f,-0x1.e0766e0000000p-1f,0x1.61d5960000000p-2f,
-0x1.dfeae60000000p-1f,0x1.64c7de0000000p-2f,-0x1.df5e360000000p-1f,0x1.67b94a0000000p-2f,-0x1.ded0600000000p-1f,0x1.6aa9d80000000p-2f,-0x1.de41600000000p-1f,0x1.6d99860000000p-2f,
-0x1.ddb13c0000000p-1f,0x1.7088540000000p-2f,-0x1.dd1ff00000000p-1f,0x1.73763c0000000p-2f,-0x1.dc8d7c0000000p-1f,0x1.7663400000000p-2f,-0x1.dbf9e40000000p-1f,0x1.794f5e0000000p-2f,
-0x1.db65260000000p-1f,0x1.7c3a940000000p-2f,-0x1.dacf420000000p-1f,0x1.7f24de0000000p-2f,-0x1.da383a0000000p-1f,0x1.820e3c0000000p-2f,-0x1.d9a00e0000000p-1f,0x1.84f6aa0000000p-2f,
-0x1.d906bc0000000p-1f,0x1.87de2a0000000p-2f,-0x1.d86c480000000p-1f,0x1.8ac4b80000000p-2f,-0x1.d7d0b00000000p-1f,0x1.8daa520000000p-2f,-0x1.d733f60000000p-1f,0x1.908ef80000000p-2f,
-0x1.d696180000000p-1f,0x1.9372a60000000p-2f,-0x1.d5f7180000000p-1f,0x1.96555c0000000p-2f,-0x1.d556f60000000p-1f,0x1.9937160000000p-2f,-0x1.d4b5b20000000p-1f,0x1.9c17d40000000p-2f,
-0x1.d4134e0000000p-1f,0x1.9ef7940000000p-2f,-0x1.d36fc80000000p-1f,0x1.a1d6540000000p-2f,-0x1.d2cb220000000p-1f,0x1.a4b4120000000p-2f,-0x1.d2255c0000000p-1f,0x1.a790ce0000000p-2f,
-0x1.d17e780000000p-1f,0x1.aa6c820000000p-2f,-0x1.d0d6720000000p-1f,0x1.ad47320000000p-2f,-0x1.d02d500000000p-1f,0x1.b020d60000000p-2f,-0x1.cf830e0000000p-1f,0x1.b2f9720000000p-2f,
-0x1.ced7b00000000p-1f,0x1.b5d1000000000p-2f,-0x1.ce2b320000000p-1f,0x1.b8a7820000000p-2f,-0x1.cd7d980000000p-1f,0x1.bb7cf20000000p-2f,-0x1.cccee20000000p-1f,0x1.be51520000000p-2f,
-0x1.cc1f100000000p-1f,0x1.c1249e0000000p-2f,-0x1.cb6e200000000p-1f,0x1.c3f6d40000000p-2f,-0x1.cabc160000000p-1f,0x1.c6c7f40000000p-2f,-0x1.ca08f20000000p-1f,0x1.c997fc0000000p-2f,
-0x1.c954b20000000p-1f,0x1.cc66ea0000000p-2f,-0x1.c89f580000000p-1f,0x1.cf34ba0000000p-2f,-0x1.c7e8e60000000p-1f,0x1.d2016e0000000p-2f,-0x1.c731580000000p-1f,0x1.d4cd020000000p-2f,
-0x1.c678b40000000p-1f,0x1.d797760000000p-2f,-0x1.c5bef60000000p-1f,0x1.da60c60000000p-2f,-0x1.c504200000000p-1f,0x1.dd28f20000000p-2f,-0x1.c448340000000p-1f,0x1.dfeff60000000p-2f,
-0x1.c38b300000000p-1f,0x1.e2b5d40000000p-2f,-0x1.c2cd140000000p-1f,0x1.e57a860000000p-2f,-0x1.c20de40000000p-1f,0x1.e83e0e0000000p-2f,-0x1.c14d9e0000000p-1f,0x1.eb006a0000000p-2f,
-0x1.c08c420000000p-1f,0x1.edc1960000000p-2f,-0x1.bfc9d20000000p-1f,0x1.f081900000000p-2f,-0x1.bf064e0000000p-1f,0x1.f3405a0000000p-2f,-0x1.be41b60000000p-1f,0x1.f5fdee0000000p-2f,
-0x1.bd7c0a0000000p-1f,0x1.f8ba4e0000000p-2f,-0x1.bcb54c0000000p-1f,0x1.fb75760000000p-2f,-0x1.bbed7c0000000p-1f,0x1.fe2f640000000p-2f,-0x1.bb249a0000000p-1f,0x1.00740c0000000p-1f,
-0x1.ba5aa60000000p-1f,0x1.01cfc80000000p-1f,-0x1.b98fa20000000p-1f,0x1.032ae60000000p-1f,-0x1.b8c38e0000000p-1f,0x1.0485620000000p-1f,-0x1.b7f6680000000p-1f,0x1.05df3e0000000p-1f,
-0x1.b728340000000p-1f,0x1.07387a0000000p-1f,-0x1.b658f20000000p-1f,0x1.0891120000000p-1f,-0x1.b588a00000000p-1f,0x1.09e9080000000p-1f,-0x1.b4b7400000000p-1f,0x1.0b40580000000p-1f,
-0x1.b3e4d40000000p-1f,0x1.0c97040000000p-1f,-0x1.b3115a0000000p-1f,0x1.0ded0c0000000p-1f,-0x1.b23cd40000000p-1f,0x1.0f426c0000000p-1f,-0x1.b167420000000p-1f,0x1.1097240000000p-1f,
-0x1.b090a60000000p-1f,0x1.11eb360000000p-1f,-0x1.afb8fe0000000p-1f,0x1.133e9c0000000p-1f,-0x1.aee04c0000000p-1f,0x1.14915a0000000p-1f,-0x1.ae06900000000p-1f,0x1.15e36e0000000p-1f,
-0x1.ad2bca0000000p-1f,0x1.1734d60000000p-1f,-0x1.ac4ffc0000000p-1f,0x1.1885920000000p-1f,-0x1.ab73260000000p-1f,0x1.19d5a00000000p-1f,-0x1.aa95480000000p-1f,0x1.1b25020000000p-1f,
-0x1.a9b6620000000p-1f,0x1.1c73b40000000p-1f,-0x1.a8d6760000000p-1f,0x1.1dc1b60000000p-1f,-0x1.a7f5860000000p-1f,0x1.1f0f080000000p-1f,-0x1.a7138e0000000p-1f,0x1.205baa0000000p-1f,
-0x1.a630920000000p-1f,0x1.21a79a0000000p-1f,-0x1.a54c920000000p-1f,0x1.22f2d60000000p-1f,-0x1.a4678c0000000p-1f,0x1.243d600000000p-1f,-0x1.a381840000000p-1f,0x1.2587340000000p-1f,
-0x1.a29a7a0000000p-1f,0x1.26d0540000000p-1f,-0x1.a1b26e0000000p-1f,0x1.2818be0000000p-1f,-0x1.a0c95e0000000p-1f,0x1.2960720000000p-1f,-0x1.9fdf500000000p-1f,0x1.2aa76e0000000p-1f,
-0x1.9ef43e0000000p-1f,0x1.2bedb20000000p-1f,-0x1.9e082e0000000p-1f,0x1.2d333e0000000p-1f,-0x1.9d1b200000000p-1f,0x1.2e780e0000000p-1f,-0x1.9c2d120000000p-1f,0x1.2fbc240000000p-1f,
-0x1.9b3e040000000p-1f,0x1.30ff800000000p-1f,-0x1.9a4dfa0000000p-1f,0x1.32421e0000000p-1f,-0x1.995cf20000000p-1f,0x1.3384000000000p-1f,-0x1.986af00000000p-1f,0x1.34c5260000000p-1f,
-0x1.9777f00000000p-1f,0x1.36058c0000000p-1f,-0x1.9683f40000000p-1f,0x1.3745320000000p-1f,-0x1.958efe0000000p-1f,0x1.3884180000000p-1f,-0x1.94990e0000000p-1f,0x1.39c23e0000000p-1f,
-0x1.93a2240000000p-1f,0x1.3affa20000000p-1f,-0x1.92aa420000000p-1f,0x1.3c3c440000000p-1f,-0x1.91b1660000000p-1f,0x1.3d78240000000p-1f,-0x1.90b7940000000p-1f,0x1.3eb33e0000000p-1f,
-0x1.8fbcca0000000p-1f,0x1.3fed960000000p-1f,-0x1.8ec10a0000000p-1f,0x1.4127260000000p-1f,-0x1.8dc4540000000p-1f,0x1.425ff20000000p-1f,-0x1.8cc6a80000000p-1f,0x1.4397f60000000p-1f,
-0x1.8bc8060000000p-1f,0x1.44cf320000000p-1f,-0x1.8ac8720000000p-1f,0x1.4605a60000000p-1f,-0x1.89c7ea0000000p-1f,0x1.473b520000000p-1f,-0x1.88c66e0000000p-1f,0x1.4870340000000p-1f,
-0x1.87c4000000000p-1f,0x1.49a44a0000000p-1f,-0x1.86c0a20000000p-1f,0x1.4ad7960000000p-1f,-0x1.85bc520000000p-1f,0x1.4c0a140000000p-1f,-0x1.84b7120000000p-1f,0x1.4d3bc60000000p-1f,
-0x1.83b0e00000000p-1f,0x1.4e6cac0000000p-1f,-0x1.82a9c20000000p-1f,0x1.4f9cc20000000p-1f,-0x1.81a1b40000000p-1f,0x1.50cc0a0000000p-1f,-0x1.8098b80000000p-1f,0x1.51fa820000000p-1f,
-0x1.7f8ece0000000p-1f,0x1.53282a0000000p-1f,-0x1.7e83f80000000p-1f,0x1.5455000000000p-1f,-0x1.7d78360000000p-1f,0x1.5581040000000p-1f,-0x1.7c6b8a0000000p-1f,0x1.56ac360000000p-1f,
-0x1.7b5df20000000p-1f,0x1.57d6940000000p-1f,-0x1.7a4f700000000p-1f,0x1.59001e0000000p-1f,-0x1.7940060000000p-1f,0x1.5a28d20000000p-1f,-0x1.782fb20000000p-1f,0x1.5b50b20000000p-1f,
-0x1.771e760000000p-1f,0x1.5c77bc0000000p-1f,-0x1.760c520000000p-1f,0x1.5d9dee0000000p-1f,-0x1.74f9480000000p-1f,0x1.5ec34a0000000p-1f,-0x1.73e5580000000p-1f,0x1.5fe7cc0000000p-1f,
-0x1.72d0840000000p-1f,0x1.610b760000000p-1f,-0x1.71baca0000000p-1f,0x1.622e440000000p-1f,-0x1.70a42c0000000p-1f,0x1.63503a0000000p-1f,-0x1.6f8caa0000000p-1f,0x1.6471540000000p-1f,
-0x1.6e74460000000p-1f,0x1.6591920000000p-1f,-0x1.6d5afe0000000p-1f,0x1.66b0f40000000p-1f,-0x1.6c40d80000000p-1f,0x1.67cf780000000p-1f,-0x1.6b25ce0000000p-1f,0x1.68ed1e0000000p-1f,
-0x1.6a09e60000000p-1f,0x1.6a09e60000000p-1f,-0x1.68ed1e0000000p-1f,0x1.6b25ce0000000p-1f,-0x1.67cf780000000p-1f,0x1.6c40d80000000p-1f,-0x1.66b0f40000000p-1f,0x1.6d5afe0000000p-1f,
-0x1.6591920000000p-1f,0x1.6e74460000000p-1f,-0x1.6471540000000p-1f,0x1.6f8caa0000000p-1f,-0x1.63503a0000000p-1f,0x1.70a42c0000000p-1f,-0x1.622e440000000p-1f,0x1.71baca0000000p-1f,
-0x1.610b760000000p-1f,0x1.72d0840000000p-1f,-0x1.5fe7cc0000000p-1f,0x1.73e5580000000p-1f,-0x1.5ec34a0000000p-1f,0x1.74f9480000000p-1f,-0x1.5d9dee0000000p-1f,0x1.760c520000000p-1f,
-0x1.5c77bc0000000p-1f,0x1.771e760000000p-1f,-0x1.5b50b20000000p-1f,0x1.782fb20000000p-1f,-0x1.5a28d20000000p-1f,0x1.7940060000000p-1f,-0x1.59001e0000000p-1f,0x1.7a4f700000000p-1f,
-0x1.57d6940000000p-1f,0x1.7b5df20000000p-1f,-0x1.56ac360000000p-1f,0x1.7c6b8a0000000p-1f,-0x1.5581040000000p-1f,0x1.7d78360000000p-1f,-0x1.5455000000000p-1f,0x1.7e83f80000000p-1f,
-0x1.53282a0000000p-1f,0x1.7f8ece0000000p-1f,-0x1.51fa820000000p-1f,0x1.8098b80000000p-1f,-0x1.50cc0a0000000p-1f,0x1.81a1b40000000p-1f,-0x1.4f9cc20000000p-1f,0x1.82a9c20000000p-1f,
-0x1.4e6cac0000000p-1f,0x1.83b0e00000000p-1f,-0x1.4d3bc60000000p-1f,0x1.84b7120000000p-1f,-0x1.4c0a140000000p-1f,0x1.85bc520000000p-1f,-0x1.4ad7960000000p-1f,0x1.86c0a20000000p-1f,
-0x1.49a44a0000000p-1f,0x1.87c4000000000p-1f,-0x1.4870340000000p-1f,0x1.88c66e0000000p-1f,-0x1.473b520000000p-1f,0x1.89c7ea0000000p-1f,-0x1.4605a60000000p-1f,0x1.8ac8720000000p-1f,
-0x1.44cf320000000p-1f,0x1.8bc8060000000p-1f,-0x1.4397f60000000p-1f,0x1.8cc6a80000000p-1f,-0x1.425ff20000000p-1f,0x1.8dc4540000000p-1f,-0x1.4127260000000p-1f,0x1.8ec10a0000000p-1f,
-0x1.3fed960000000p-1f,0x1.8fbcca0000000p-1f,-0x1.3eb33e0000000p-1f,0x1.90b7940000000p-1f,-0x1.3d78240000000p-1f,0x1.91b1660000000p-1f,-0x1.3c3c440000000p-1f,0x1.92aa420000000p-1f,
-0x1.3affa20000000p-1f,0x1.93a2240000000p-1f,-0x1.39c23e0000000p-1f,0x1.94990e0000000p-1f,-0x1.3884180000000p-1f,0x1.958efe0000000p-1f,-0x1.3745320000000p-1f,0x1.9683f40000000p-1f,
-0x1.36058c0000000p-1f,0x1.9777f00000000p-1f,-0x1.34c5260000000p-1f,0x1.986af00000000p-1f,-0x1.3384000000000p-1f,0x1.995cf20000000p-1f,-0x1.32421e0000000p-1f,0x1.9a4dfa0000000p-1f,
-0x1.30ff800000000p-1f,0x1.9b3e040000000p-1f,-0x1.2fbc240000000p-1f,0x1.9c2d120000000p-1f,-0x1.2e780e0000000p-1f,0x1.9d1b200000000p-1f,-0x1.2d333e0000000p-1f,0x1.9e082e0000000p-1f,
-0x1.2bedb20000000p-1f,0x1.9ef43e0000000p-1f,-0x1.2aa76e0000000p-1f,0x1.9fdf500000000p-1f,-0x1.2960720000000p-1f,0x1.a0c95e0000000p-1f,-0x1.2818be0000000p-1f,0x1.a1b26e0000000p-1f,
-0x1.26d0540000000p-1f,0x1.a29a7a0000000p-1f,-0x1.2587340000000p-1f,0x1.a381840000000p-1f,-0x1.243d600000000p-1f,0x1.a4678c0000000p-1f,-0x1.22f2d60000000p-1f,0x1.a54c920000000p-1f,
-0x1.21a79a0000000p-1f,0x1.a630920000000p-1f,-0x1.205baa0000000p-1f,0x1.a7138e0000000p-1f,-0x1.1f0f080000000p-1f,0x1.a7f5860000000p-1f,-0x1.1dc1b60000000p-1f,0x1.a8d6760000000p-1f,
-0x1.1c73b40000000p-1f,0x1.a9b6620000000p-1f,-0x1.1b25020000000p-1f,0x1.aa95480000000p-1f,-0x1.19d5a00000000p-1f,0x1.ab73260000000p-1f,-0x1.1885920000000p-1f,0x1.ac4ffc0000000p-1f,
-0x1.1734d60000000p-1f,0x1.ad2bca0000000p-1f,-0x1.15e36e0000000p-1f,0x1.ae06900000000p-1f,-0x1.14915a0000000p-1f,0x1.aee04c0000000p-1f,-0x1.133e9c0000000p-1f,0x1.afb8fe0000000p-1f,
-0x1.11eb360000000p-1f,0x1.b090a60000000p-1f,-0x1.1097240000000p-1f,0x1.b167420000000p-1f,-0x1.0f426c0000000p-1f,0x1.b23cd40000000p-1f,-0x1.0ded0c0000000p-1f,0x1.b3115a0000000p-1f,
-0x1.0c97040000000p-1f,0x1.b3e4d40000000p-1f,-0x1.0b40580000000p-1f,0x1.b4b7400000000p-1f,-0x1.09e9080000000p-1f,0x1.b588a00000000p-1f,-0x1.0891120000000p-1f,0x1.b658f20000000p-1f,
-0x1.07387a0000000p-1f,0x1.b728340000000p-1f,-0x1.05df3e0000000p-1f,0x1.b7f6680000000p-1f,-0x1.0485620000000p-1f,0x1.b8c38e0000000p-1f,-0x1.032ae60000000p-1f,0x1.b98fa20000000p-1f,
-0x1.01cfc80000000p-1f,0x1.ba5aa60000000p-1f,-0x1.00740c0000000p-1f,0x1.bb249a0000000p-1f,-0x1.fe2f640000000p-2f,0x1.bbed7c0000000p-1f,-0x1.fb75760000000p-2f,0x1.bcb54c0000000p-1f,
-0x1.f8ba4e0000000p-2f,0x1.bd7c0a0000000p-1f,-0x1.f5fdee0000000p-2f,0x1.be41b60000000p-1f,-0x1.f3405a0000000p-2f,0x1.bf064e0000000p-1f,-0x1.f081900000000p-2f,0x1.bfc9d20000000p-1f,
-0x1.edc1960000000p-2f,0x1.c08c420000000p-1f,-0x1.eb006a0000000p-2f,0x1.c14d9e0000000p-1f,-0x1.e83e0e0000000p-2f,0x1.c20de40000000p-1f,-0x1.e57a860000000p-2f,0x1.c2cd140000000p-1f,
-0x1.e2b5d40000000p-2f,0x1.c38b300000000p-1f,-0x1.dfeff60000000p-2f,0x1.c448340000000p-1f,-0x1.dd28f20000000p-2f,0x1.c504200000000p-1f,-0x1.da60c60000000p-2f,0x1.c5bef60000000p-1f,
-0x1.d797760000000p-2f,0x1.c678b40000000p-1f,-0x1.d4cd020000000p-2f,0x1.c731580000000p-1f,-0x1.d2016e0000000p-2f,0x1.c7e8e60000000p-1f,-0x1.cf34ba0000000p-2f,0x1.c89f580000000p-1f,
-0x1.cc66ea0000000p-2f,0x1.c954b20000000p-1f,-0x1.c997fc0000000p-2f,0x1.ca08f20000000p-1f,-0x1.c6c7f40000000p-2f,0x1.cabc160000000p-1f,-0x1.c3f6d40000000p-2f,0x1.cb6e200000000p-1f,
-0x1.c1249e0000000p-2f,0x1.cc1f100000000p-1f,-0x1.be51520000000p-2f,0x1.cccee20000000p-1f,-0x1.bb7cf20000000p-2f,0x1.cd7d980000000p-1f,-0x1.b8a7820000000p-2f,0x1.ce2b320000000p-1f,
-0x1.b5d1000000000p-2f,0x1.ced7b00000000p-1f,-0x1.b2f9720000000p-2f,0x1.cf830e0000000p-1f,-0x1.b020d60000000p-2f,0x1.d02d500000000p-1f,-0x1.ad47320000000p-2f,0x1.d0d6720000000p-1f,
-0x1.aa6c820000000p-2f,0x1.d17e780000000p-1f,-0x1.a790ce0000000p-2f,0x1.d2255c0000000p-1f,-0x1.a4b4120000000p-2f,0x1.d2cb220000000p-1f,-0x1.a1d6540000000p-2f,0x1.d36fc80000000p-1f,
-0x1.9ef7940000000p-2f,0x1.d4134e0000000p-1f,-0x1.9c17d40000000p-2f,0x1.d4b5b20000000p-1f,-0x1.9937160000000p-2f,0x1.d556f60000000p-1f,-0x1.96555c0000000p-2f,0x1.d5f7180000000p-1f,
-0x1.9372a60000000p-2f,0x1.d696180000000p-1f,-0x1.908ef80000000p-2f,0x1.d733f60000000p-1f,-0x1.8daa520000000p-2f,0x1.d7d0b00000000p-1f,-0x1.8ac4b80000000p-2f,0x1.d86c480000000p-1f,
-0x1.87de2a0000000p-2f,0x1.d906bc0000000p-1f,-0x1.84f6aa0000000p-2f,0x1.d9a00e0000000p-1f,-0x1.820e3c0000000p-2f,0x1.da383a0000000p-1f,-0x1.7f24de0000000p-2f,0x1.dacf420000000p-1f,
-0x1.7c3a940000000p-2f,0x1.db65260000000p-1f,-0x1.794f5e0000000p-2f,0x1.dbf9e40000000p-1f,-0x1.7663400000000p-2f,0x1.dc8d7c0000000p-1f,-0x1.73763c0000000p-2f,0x1.dd1ff00000000p-1f,
-0x1.7088540000000p-2f,0x1.ddb13c0000000p-1f,-0x1.6d99860000000p-2f,0x1.de41600000000p-1f,-0x1.6aa9d80000000p-2f,0x1.ded0600000000p-1f,-0x1.67b94a0000000p-2f,0x1.df5e360000000p-1f,
-0x1.64c7de0000000p-2f,0x1.dfeae60000000p-1f,-0x1.61d5960000000p-2f,0x1.e0766e0000000p-1f,-0x1.5ee2740000000p-2f,0x1.e100cc0000000p-1f,-0x1.5bee780000000p-2f,0x1.e18a020000000p-1f,
-0x1.58f9a80000000p-2f,0x1.e212100000000p-1f,-0x1.5604020000000p-2f,0x1.e298f40000000p-1f,-0x1.530d880000000p-2f,0x1.e31eae0000000p-1f,-0x1.50163e0000000p-2f,0x1.e3a33e0000000p-1f,
-0x1.4d1e240000000p-2f,0x1.e426a40000000p-1f,-0x1.4a253e0000000p-2f,0x1.e4a8e00000000p-1f,-0x1.472b8a0000000p-2f,0x1.e529f00000000p-1f,-0x1.44310e0000000p-2f,0x1.e5a9d60000000p-1f,
-0x1.4135ca0000000p-2f,0x1.e6288e0000000p-1f,-0x1.3e39be0000000p-2f,0x1.e6a61c0000000p-1f,-0x1.3b3cf00000000p-2f,0x1.e7227e0000000p-1f,-0x1.383f5e0000000p-2f,0x1.e79db20000000p-1f,
-0x1.35410c0000000p-2f,0x1.e817ba0000000p-1f,-0x1.3241fc0000000p-2f,0x1.e890960000000p-1f,-0x1.2f422e0000000p-2f,0x1.e908440000000p-1f,-0x1.2c41a40000000p-2f,0x1.e97ec40000000p-1f,
-0x1.2940620000000p-2f,0x1.e9f4160000000p-1f,-0x1.263e6a0000000p-2f,0x1.ea683a0000000p-1f,-0x1.233bba0000000p-2f,0x1.eadb2e0000000p-1f,-0x1.2038580000000p-2f,0x1.eb4cf60000000p-1f,
-0x1.1d34440000000p-2f,0x1.ebbd8c0000000p-1f,-0x1.1a2f800000000p-2f,0x1.ec2cf40000000p-1f,-0x1.172a0e0000000p-2f,0x1.ec9b2e0000000p-1f,-0x1.1423ee0000000p-2f,0x1.ed08360000000p-1f,
-0x1.111d260000000p-2f,0x1.ed740e0000000p-1f,-0x1.0e15b40000000p-2f,0x1.eddeb60000000p-1f,-0x1.0b0d9c0000000p-2f,0x1.ee482e0000000p-1f,-0x1.0804e00000000p-2f,0x1.eeb0740000000p-1f,
-0x1.04fb800000000p-2f,0x1.ef178a0000000p-1f,-0x1.01f1800000000p-2f,0x1.ef7d6e0000000p-1f,-0x1.fdcdc20000000p-3f,0x1.efe2200000000p-1f,-0x1.f7b7480000000p-3f,0x1.f045a20000000p-1f,
-0x1.f19f980000000p-3f,0x1.f0a7f00000000p-1f,-0x1.eb86b40000000p-3f,0x1.f1090c0000000p-1f,-0x1.e56ca20000000p-3f,0x1.f168f60000000p-1f,-0x1.df51640000000p-3f,0x1.f1c7ac0000000p-1f,
-0x1.d934fe0000000p-3f,0x1.f225300000000p-1f,-0x1.d317740000000p-3f,0x1.f281800000000p-1f,-0x1.ccf8cc0000000p-3f,0x1.f2dc9c0000000p-1f,-0x1.c6d9060000000p-3f,0x1.f336860000000p-1f,
-0x1.c0b8260000000p-3f,0x1.f38f3a0000000p-1f,-0x1.ba96340000000p-3f,0x1.f3e6bc0000000p-1f,-0x1.b4732e0000000p-3f,0x1.f43d080000000p-1f,-0x1.ae4f1e0000000p-3f,0x1.f492200000000p-1f,
-0x1.a82a020000000p-3f,0x1.f4e6040000000p-1f,-0x1.a203e20000000p-3f,0x1.f538b20000000p-1f,-0x1.9bdcc00000000p-3f,0x1.f58a2c0000000p-1f,-0x1.95b49e0000000p-3f,0x1.f5da6e0000000p-1f,
-0x1.8f8b840000000p-3f,0x1.f6297c0000000p-1f,-0x1.8961720000000p-3f,0x1.f677560000000p-1f,-0x1.83366e0000000p-3f,0x1.f6c3f80000000p-1f,-0x1.7d0a7c0000000p-3f,0x1.f70f640000000p-1f,
-0x1.76dd9e0000000p-3f,0x1.f7599a0000000p-1f,-0x1.70afd80000000p-3f,0x1.f7a29a0000000p-1f,-0x1.6a81300000000p-3f,0x1.f7ea620000000p-1f,-0x1.6451a80000000p-3f,0x1.f830f40000000p-1f,
-0x1.5e21440000000p-3f,0x1.f876500000000p-1f,-0x1.57f0080000000p-3f,0x1.f8ba740000000p-1f,-0x1.51bdf80000000p-3f,0x1.f8fd600000000p-1f,-0x1.4b8b180000000p-3f,0x1.f93f140000000p-1f,
-0x1.45576c0000000p-3f,0x1.f97f920000000p-1f,-0x1.3f22f60000000p-3f,0x1.f9bed80000000p-1f,-0x1.38edbc0000000p-3f,0x1.f9fce60000000p-1f,-0x1.32b7c00000000p-3f,0x1.fa39ba0000000p-1f,
-0x1.2c81060000000p-3f,0x1.fa75580000000p-1f,-0x1.2649940000000p-3f,0x1.faafbc0000000p-1f,-0x1.20116e0000000p-3f,0x1.fae8e80000000p-1f,-0x1.19d8940000000p-3f,0x1.fb20dc0000000p-1f,
-0x1.139f0c0000000p-3f,0x1.fb57980000000p-1f,-0x1.0d64dc0000000p-3f,0x1.fb8d180000000p-1f,-0x1.072a040000000p-3f,0x1.fbc1620000000p-1f,-0x1.00ee8a0000000p-3f,0x1.fbf4700000000p-1f,
-0x1.f564e60000000p-4f,0x1.fc26480000000p-1f,-0x1.e8eb800000000p-4f,0x1.fc56e40000000p-1f,-0x1.dc70ec0000000p-4f,0x1.fc86460000000p-1f,-0x1.cff5340000000p-4f,0x1.fcb4700000000p-1f,
-0x1.c3785c0000000p-4f,0x1.fce1600000000p-1f,-0x1.b6fa6e0000000p-4f,0x1.fd0d160000000p-1f,-0x1.aa7b720000000p-4f,0x1.fd37920000000p-1f,-0x1.9dfb6e0000000p-4f,0x1.fd60d20000000p-1f,
-0x1.917a6c0000000p-4f,0x1.fd88da0000000p-1f,-0x1.84f8720000000p-4f,0x1.fdafa80000000p-1f,-0x1.7875860000000p-4f,0x1.fdd53a0000000p-1f,-0x1.6bf1b40000000p-4f,0x1.fdf9920000000p-1f,
-0x1.5f6d000000000p-4f,0x1.fe1cb00000000p-1f,-0x1.52e7740000000p-4f,0x1.fe3e920000000p-1f,-0x1.4661180000000p-4f,0x1.fe5f3a0000000p-1f,-0x1.39d9f20000000p-4f,0x1.fe7ea80000000p-1f,
-0x1.2d520a0000000p-4f,0x1.fe9cda0000000p-1f,-0x1.20c9680000000p-4f,0x1.feb9d20000000p-1f,-0x1.1440140000000p-4f,0x1.fed58e0000000p-1f,-0x1.07b6140000000p-4f,0x1.fef0100000000p-1f,
-0x1.f656e80000000p-5f,0x1.ff09560000000p-1f,-0x1.dd40700000000p-5f,0x1.ff21620000000p-1f,-0x1.c428d20000000p-5f,0x1.ff38300000000p-1f,-0x1.ab101c0000000p-5f,0x1.ff4dc60000000p-1f,
-0x1.91f6600000000p-5f,0x1.ff621e0000000p-1f,-0x1.78dbaa0000000p-5f,0x1.ff753c0000000p-1f,-0x1.5fc00e0000000p-5f,0x1.ff871e0000000p-1f,-0x1.46a3960000000p-5f,0x1.ff97c40000000p-1f,
-0x1.2d86580000000p-5f,0x1.ffa72e0000000p-1f,-0x1.14685e0000000p-5f,0x1.ffb55e0000000p-1f,-0x1.f693740000000p-6f,0x1.ffc2520000000p-1f,-0x1.c454f40000000p-6f,0x1.ffce0a0000000p-1f,
-0x1.9215600000000p-6f,0x1.ffd8860000000p-1f,-0x1.5fd4d20000000p-6f,0x1.ffe1c60000000p-1f,-0x1.2d936c0000000p-6f,0x1.ffe9cc0000000p-1f,-0x1.f6a2960000000p-7f,0x1.fff0940000000p-1f,
-0x1.921d200000000p-7f,0x1.fff6220000000p-1f,-0x1.2d96b00000000p-7f,0x1.fffa720000000p-1f,-0x1.921f100000000p-8f,0x1.fffd880000000p-1f,-0x1.921f8c0000000p-9f,0x1.ffff620000000p-1f,
0.0f,0x1.0000000000000p+0f,0x1.921f8c0000000p-9f,0x1.ffff620000000p-1f,0x1.921f100000000p-8f,0x1.fffd880000000p-1f,0x1.2d96b00000000p-7f,0x1.fffa720000000p-1f,
0x1.921d200000000p-7f,0x1.fff6220000000p-1f,0x1.f6a2960000000p-7f,0x1.fff0940000000p-1f,0x1.2d936c0000000p-6f,0x1.ffe9cc0000000p-1f,0x1.5fd4d20000000p-6f,0x1.ffe1c60000000p-1f,
0x1.9215600000000p-6f,0x1.ffd8860000000p-1f,0x1.c454f40000000p-6f,0x1.ffce0a0000000p-1f,0x1.f693740000000p-6f,0x1.ffc2520000000p-1f,0x1.14685e0000000p-5f,0x1.ffb55e0000000p-1f,
0x1.2d86580000000p-5f,0x1.ffa72e0000000p-1f,0x1.46a3960000000p-5f,0x1.ff97c40000000p-1f,0x1.5fc00e0000000p-5f,0x1.ff871e0000000p-1f,0x1.78dbaa0000000p-5f,0x1.ff753c0000000p-1f,
0x1.91f6600000000p-5f,0x1.ff621e0000000p-1f,0x1.ab101c0000000p-5f,0x1.ff4dc60000000p-1f,0x1.c428d20000000p-5f,0x1.ff38300000000p-1f,0x1.dd40700000000p-5f,0x1.ff21620000000p-1f,
0x1.f656e80000000p-5f,0x1.ff09560000000p-1f,0x1.07b6140000000p-4f,0x1.fef0100000000p-1f,0x1.1440140000000p-4f,0x1.fed58e0000000p-1f,0x1.20c9680000000p-4f,0x1.feb9d20000000p-1f,
0x1.2d520a0000000p-4f,0x1.fe9cda0000000p-1f,0x1.39d9f20000000p-4f,0x1.fe7ea80000000p-1f,0x1.4661180000000p-4f,0x1.fe5f3a0000000p-1f,0x1.52e7740000000p-4f,0x1.fe3e920000000p-1f,
0x1.5f6d000000000p-4f,0x1.fe1cb00000000p-1f,0x1.6bf1b40000000p-4f,0x1.fdf9920000000p-1f,0x1.7875860000000p-4f,0x1.fdd53a0000000p-1f,0x1.84f8720000000p-4f,0x1.fdafa80000000p-1f,
0x1.917a6c0000000p-4f,0x1.fd88da0000000p-1f,0x1.9dfb6e0000000p-4f,0x1.fd60d20000000p-1f,0x1.aa7b720000000p-4f,0x1.fd37920000000p-1f,0x1.b6fa6e0000000p-4f,0x1.fd0d160000000p-1f,
0x1.c3785c0000000p-4f,0x1.fce1600000000p-1f,0x1.cff5340000000p-4f,0x1.fcb4700000000p-1f,0x1.dc70ec0000000p-4f,0x1.fc86460000000p-1f,0x1.e8eb800000000p-4f,0x1.fc56e40000000p-1f,
0x1.f564e60000000p-4f,0x1.fc26480000000p-1f,0x1.00ee8a0000000p-3f,0x1.fbf4700000000p-1f,0x1.072a040000000p-3f,0x1.fbc1620000000p-1f,0x1.0d64dc0000000p-3f,0x1.fb8d180000000p-1f,
0x1.139f0c0000000p-3f,0x1.fb57980000000p-1f,0x1.19d8940000000p-3f,0x1.fb20dc0000000p-1f,0x1.20116e0000000p-3f,0x1.fae8e80000000p-1f,0x1.2649940000000p-3f,0x1.faafbc0000000p-1f,
0x1.2c81060000000p-3f,0x1.fa75580000000p-1f,0x1.32b7c00000000p-3f,0x1.fa39ba0000000p-1f,0x1.38edbc0000000p-3f,0x1.f9fce60000000p-1f,0x1.3f22f60000000p-3f,0x1.f9bed80000000p-1f,
0x1.45576c0000000p-3f,0x1.f97f920000000p-1f,0x1.4b8b180000000p-3f,0x1.f93f140000000p-1f,0x1.51bdf80000000p-3f,0x1.f8fd600000000p-1f,0x1.57f0080000000p-3f,0x1.f8ba740000000p-1f,
0x1.5e21440000000p-3f,0x1.f876500000000p-1f,0x1.6451a80000000p-3f,0x1.f830f40000000p-1f,0x1.6a81300000000p-3f,0x1.f7ea620000000p-1f,0x1.70afd80000000p-3f,0x1.f7a29a0000000p-1f,
0x1.76dd9e0000000p-3f,0x1.f7599a0000000p-1f,0x1.7d0a7c0000000p-3f,0x1.f70f640000000p-1f,0x1.83366e0000000p-3f,0x1.f6c3f80000000p-1f,0x1.8961720000000p-3f,0x1.f677560000000p-1f,
0x1.8f8b840000000p-3f,0x1.f6297c0000000p-1f,0x1.95b49e0000000p-3f,0x1.f5da6e0000000p-1f,0x1.9bdcc00000000p-3f,0x1.f58a2c0000000p-1f,0x1.a203e20000000p-3f,0x1.f538b20000000p-1f,
0x1.a82a020000000p-3f,0x1.f4e6040000000p-1f,0x1.ae4f1e0000000p-3f,0x1.f492200000000p-1f,0x1.b4732e0000000p-3f,0x1.f43d080000000p-1f,0x1.ba96340000000p-3f,0x1.f3e6bc0000000p-1f,
0x1.c0b8260000000p-3f,0x1.f38f3a0000000p-1f,0x1.c6d9060000000p-3f,0x1.f336860000000p-1f,0x1.ccf8cc0000000p-3f,0x1.f2dc9c0000000p-1f,0x1.d317740000000p-3f,0x1.f281800000000p-1f,
0x1.d934fe0000000p-3f,0x1.f225300000000p-1f,0x1.df51640000000p-3f,0x1.f1c7ac0000000p-1f,0x1.e56ca20000000p-3f,0x1.f168f60000000p-1f,0x1.eb86b40000000p-3f,0x1.f1090c0000000p-1f,
0x1.f19f980000000p-3f,0x1.f0a7f00000000p-1f,0x1.f7b7480000000p-3f,0x1.f045a20000000p-1f,0x1.fdcdc20000000p-3f,0x1.efe2200000000p-1f,0x1.01f1800000000p-2f,0x1.ef7d6e0000000p-1f,
0x1.04fb800000000p-2f,0x1.ef178a0000000p-1f,0x1.0804e00000000p-2f,0x1.eeb0740000000p-1f,0x1.0b0d9c0000000p-2f,0x1.ee482e0000000p-1f,0x1.0e15b40000000p-2f,0x1.eddeb60000000p-1f,
0x1.111d260000000p-2f,0x1.ed740e0000000p-1f,0x1.1423ee0000000p-2f,0x1.ed08360000000p-1f,0x1.172a0e0000000p-2f,0x1.ec9b2e0000000p-1f,0x1.1a2f800000000p-2f,0x1.ec2cf40000000p-1f,
0x1.1d34440000000p-2f,0x1.ebbd8c0000000p-1f,0x1.2038580000000p-2f,0x1.eb4cf60000000p-1f,0x1.233bba0000000p-2f,0x1.eadb2e0000000p-1f,0x1.263e6a0000000p-2f,0x1.ea683a0000000p-1f,
0x1.2940620000000p-2f,0x1.e9f4160000000p-1f,0x1.2c41a40000000p-2f,0x1.e97ec40000000p-1f,0x1.2f422e0000000p-2f,0x1.e908440000000p-1f,0x1.3241fc0000000p-2f,0x1.e890960000000p-1f,
0x1.35410c0000000p-2f,0x1.e817ba0000000p-1f,0x1.383f5e0000000p-2f,0x1.e79db20000000p-1f,0x1.3b3cf00000000p-2f,0x1.e7227e0000000p-1f,0x1.3e39be0000000p-2f,0x1.e6a61c0000000p-1f,
0x1.4135ca0000000p-2f,0x1.e6288e0000000p-1f,0x1.44310e0000000p-2f,0x1.e5a9d60000000p-1f,0x1.472b8a0000000p-2f,0x1.e529f00000000p-1f,0x1.4a253e0000000p-2f,0x1.e4a8e00000000p-1f,
0x1.4d1e240000000p-2f,0x1.e426a40000000p-1f,0x1.50163e0000000p-2f,0x1.e3a33e0000000p-1f,0x1.530d880000000p-2f,0x1.e31eae0000000p-1f,0x1.5604020000000p-2f,0x1.e298f40000000p-1f,
0x1.58f9a80000000p-2f,0x1.e212100000000p-1f,0x1.5bee780000000p-2f,0x1.e18a020000000p-1f,0x1.5ee2740000000p-2f,0x1.e100cc0000000p-1f,0x1.61d5960000000p-2f,0x1.e0766e0000000p-1f,
0x1.64c7de0000000p-2f,0x1.dfeae60000000p-1f,0x1.67b94a0000000p-2f,0x1.df5e360000000p-1f,0x1.6aa9d80000000p-2f,0x1.ded0600000000p-1f,0x1.6d99860000000p-2f,0x1.de41600000000p-1f,
0x1.7088540000000p-2f,0x1.ddb13c0000000p-1f,0x1.73763c0000000p-2f,0x1.dd1ff00000000p-1f,0x1.7663400000000p-2f,0x1.dc8d7c0000000p-1f,0x1.794f5e0000000p-2f,0x1.dbf9e40000000p-1f,
0x1.7c3a940000000p-2f,0x1.db65260000000p-1f,0x1.7f24de0000000p-2f,0x1.dacf420000000p-1f,0x1.820e3c0000000p-2f,0x1.da383a0000000p-1f,0x1.84f6aa0000000p-2f,0x1.d9a00e0000000p-1f,
0x1.87de2a0000000p-2f,0x1.d906bc0000000p-1f,0x1.8ac4b80000000p-2f,0x1.d86c480000000p-1f,0x1.8daa520000000p-2f,0x1.d7d0b00000000p-1f,0x1.908ef80000000p-2f,0x1.d733f60000000p-1f,
0x1.9372a60000000p-2f,0x1.d696180000000p-1f,0x1.96555c0000000p-2f,0x1.d5f7180000000p-1f,0x1.9937160000000p-2f,0x1.d556f60000000p-1f,0x1.9c17d40000000p-2f,0x1.d4b5b20000000p-1f,
0x1.9ef7940000000p-2f,0x1.d4134e0000000p-1f,0x1.a1d6540000000p-2f,0x1.d36fc80000000p-1f,0x1.a4b4120000000p-2f,0x1.d2cb220000000p-1f,0x1.a790ce0000000p-2f,0x1.d2255c0000000p-1f,
0x1.aa6c820000000p-2f,0x1.d17e780000000p-1f,0x1.ad47320000000p-2f,0x1.d0d6720000000p-1f,0x1.b020d60000000p-2f,0x1.d02d500000000p-1f,0x1.b2f9720000000p-2f,0x1.cf830e0000000p-1f,
0x1.b5d1000000000p-2f,0x1.ced7b00000000p-1f,0x1.b8a7820000000p-2f,0x1.ce2b320000000p-1f,0x1.bb7cf20000000p-2f,0x1.cd7d980000000p-1f,0x1.be51520000000p-2f,0x1.cccee20000000p-1f,
0x1.c1249e0000000p-2f,0x1.cc1f100000000p-1f,0x1.c3f6d40000000p-2f,0x1.cb6e200000000p-1f,0x1.c6c7f40000000p-2f,0x1.cabc160000000p-1f,0x1.c997fc0000000p-2f,0x1.ca08f20000000p-1f,
0x1.cc66ea0000000p-2f,0x1.c954b20000000p-1f,0x1.cf34ba0000000p-2f,0x1.c89f580000000p-1f,0x1.d2016e0000000p-2f,0x1.c7e8e60000000p-1f,0x1.d4cd020000000p-2f,0x1.c731580000000p-1f,
0x1.d797760000000p-2f,0x1.c678b40000000p-1f,0x1.da60c60000000p-2f,0x1.c5bef60000000p-1f,0x1.dd28f20000000p-2f,0x1.c504200000000p-1f,0x1.dfeff60000000p-2f,0x1.c448340000000p-1f,
0x1.e2b5d40000000p-2f,0x1.c38b300000000p-1f,0x1.e57a860000000p-2f,0x1.c2cd140000000p-1f,0x1.e83e0e0000000p-2f,0x1.c20de40000000p-1f,0x1.eb006a0000000p-2f,0x1.c14d9e0000000p-1f,
0x1.edc1960000000p-2f,0x1.c08c420000000p-1f,0x1.f081900000000p-2f,0x1.bfc9d20000000p-1f,0x1.f3405a0000000p-2f,0x1.bf064e0000000p-1f,0x1.f5fdee0000000p-2f,0x1.be41b60000000p-1f,
0x1.f8ba4e0000000p-2f,0x1.bd7c0a0000000p-1f,0x1.fb75760000000p-2f,0x1.bcb54c0000000p-1f,0x1.fe2f640000000p-2f,0x1.bbed7c0000000p-1f,0x1.00740c0000000p-1f,0x1.bb249a0000000p-1f,
0x1.01cfc80000000p-1f,0x1.ba5aa60000000p-1f,0x1.032ae60000000p-1f,0x1.b98fa20000000p-1f,0x1.0485620000000p-1f,0x1.b8c38e0000000p-1f,0x1.05df3e0000000p-1f,0x1.b7f6680000000p-1f,
0x1.07387a0000000p-1f,0x1.b728340000000p-1f,0x1.0891120000000p-1f,0x1.b658f20000000p-1f,0x1.09e9080000000p-1f,0x1.b588a00000000p-1f,0x1.0b40580000000p-1f,0x1.b4b7400000000p-1f,
0x1.0c97040000000p-1f,0x1.b3e4d40000000p-1f,0x1.0ded0c0000000p-1f,0x1.b3115a0000000p-1f,0x1.0f426c0000000p-1f,0x1.b23cd40000000p-1f,0x1.1097240000000p-1f,0x1.b167420000000p-1f,
0x1.11eb360000000p-1f,0x1.b090a60000000p-1f,0x1.133e9c0000000p-1f,0x1.afb8fe0000000p-1f,0x1.14915a0000000p-1f,0x1.aee04c0000000p-1f,0x1.15e36e0000000p-1f,0x1.ae06900000000p-1f,
0x1.1734d60000000p-1f,0x1.ad2bca0000000p-1f,0x1.1885920000000p-1f,0x1.ac4ffc0000000p-1f,0x1.19d5a00000000p-1f,0x1.ab73260000000p-1f,0x1.1b25020000000p-1f,0x1.aa95480000000p-1f,
0x1.1c73b40000000p-1f,0x1.a9b6620000000p-1f,0x1.1dc1b60000000p-1f,0x1.a8d6760000000p-1f,0x1.1f0f080000000p-1f,0x1.a7f5860000000p-1f,0x1.205baa0000000p-1f,0x1.a7138e0000000p-1f,
0x1.21a79a0000000p-1f,0x1.a630920000000p-1f,0x1.22f2d60000000p-1f,0x1.a54c920000000p-1f,0x1.243d600000000p-1f,0x1.a4678c0000000p-1f,0x1.2587340000000p-1f,0x1.a381840000000p-1f,
0x1.26d0540000000p-1f,0x1.a29a7a0000000p-1f,0x1.2818be0000000p-1f,0x1.a1b26e0000000p-1f,0x1.2960720000000p-1f,0x1.a0c95e0000000p-1f,0x1.2aa76e0000000p-1f,0x1.9fdf500000000p-1f,
0x1.2bedb20000000p-1f,0x1.9ef43e0000000p-1f,0x1.2d333e0000000p-1f,0x1.9e082e0000000p-1f,0x1.2e780e0000000p-1f,0x1.9d1b200000000p-1f,0x1.2fbc240000000p-1f,0x1.9c2d120000000p-1f,
0x1.30ff800000000p-1f,0x1.9b3e040000000p-1f,0x1.32421e0000000p-1f,0x1.9a4dfa0000000p-1f,0x1.3384000000000p-1f,0x1.995cf20000000p-1f,0x1.34c5260000000p-1f,0x1.986af00000000p-1f,
0x1.36058c0000000p-1f,0x1.9777f00000000p-1f,0x1.3745320000000p-1f,0x1.9683f40000000p-1f,0x1.3884180000000p-1f,0x1.958efe0000000p-1f,0x1.39c23e0000000p-1f,0x1.94990e0000000p-1f,
0x1.3affa20000000p-1f,0x1.93a2240000000p-1f,0x1.3c3c440000000p-1f,0x1.92aa420000000p-1f,0x1.3d78240000000p-1f,0x1.91b1660000000p-1f,0x1.3eb33e0000000p-1f,0x1.90b7940000000p-1f,
0x1.3fed960000000p-1f,0x1.8fbcca0000000p-1f,0x1.4127260000000p-1f,0x1.8ec10a0000000p-1f,0x1.425ff20000000p-1f,0x1.8dc4540000000p-1f,0x1.4397f60000000p-1f,0x1.8cc6a80000000p-1f,
0x1.44cf320000000p-1f,0x1.8bc8060000000p-1f,0x1.4605a60000000p-1f,0x1.8ac8720000000p-1f,0x1.473b520000000p-1f,0x1.89c7ea0000000p-1f,0x1.4870340000000p-1f,0x1.88c66e0000000p-1f,
0x1.49a44a0000000p-1f,0x1.87c4000000000p-1f,0x1.4ad7960000000p-1f,0x1.86c0a20000000p-1f,0x1.4c0a140000000p-1f,0x1.85bc520000000p-1f,0x1.4d3bc60000000p-1f,0x1.84b7120000000p-1f,
0x1.4e6cac0000000p-1f,0x1.83b0e00000000p-1f,0x1.4f9cc20000000p-1f,0x1.82a9c20000000p-1f,0x1.50cc0a0000000p-1f,0x1.81a1b40000000p-1f,0x1.51fa820000000p-1f,0x1.8098b80000000p-1f,
0x1.53282a0000000p-1f,0x1.7f8ece0000000p-1f,0x1.5455000000000p-1f,0x1.7e83f80000000p-1f,0x1.5581040000000p-1f,0x1.7d78360000000p-1f,0x1.56ac360000000p-1f,0x1.7c6b8a0000000p-1f,
0x1.57d6940000000p-1f,0x1.7b5df20000000p-1f,0x1.59001e0000000p-1f,0x1.7a4f700000000p-1f,0x1.5a28d20000000p-1f,0x1.7940060000000p-1f,0x1.5b50b20000000p-1f,0x1.782fb20000000p-1f,
0x1.5c77bc0000000p-1f,0x1.771e760000000p-1f,0x1.5d9dee0000000p-1f,0x1.760c520000000p-1f,0x1.5ec34a0000000p-1f,0x1.74f9480000000p-1f,0x1.5fe7cc0000000p-1f,0x1.73e5580000000p-1f,
0x1.610b760000000p-1f,0x1.72d0840000000p-1f,0x1.622e440000000p-1f,0x1.71baca0000000p-1f,0x1.63503a0000000p-1f,0x1.70a42c0000000p-1f,0x1.6471540000000p-1f,0x1.6f8caa0000000p-1f,
0x1.6591920000000p-1f,0x1.6e74460000000p-1f,0x1.66b0f40000000p-1f,0x1.6d5afe0000000p-1f,0x1.67cf780000000p-1f,0x1.6c40d80000000p-1f,0x1.68ed1e0000000p-1f,0x1.6b25ce0000000p-1f,
0x1.6a09e60000000p-1f,0x1.6a09e60000000p-1f,0x1.6b25ce0000000p-1f,0x1.68ed1e0000000p-1f,0x1.6c40d80000000p-1f,0x1.67cf780000000p-1f,0x1.6d5afe0000000p-1f,0x1.66b0f40000000p-1f,
0x1.6e74460000000p-1f,0x1.6591920000000p-1f,0x1.6f8caa0000000p-1f,0x1.6471540000000p-1f,0x1.70a42c0000000p-1f,0x1.63503a0000000p-1f,0x1.71baca0000000p-1f,0x1.622e440000000p-1f,
0x1.72d0840000000p-1f,0x1.610b760000000p-1f,0x1.73e5580000000p-1f,0x1.5fe7cc0000000p-1f,0x1.74f9480000000p-1f,0x1.5ec34a0000000p-1f,0x1.760c520000000p-1f,0x1.5d9dee0000000p-1f,
0x1.771e760000000p-1f,0x1.5c77bc0000000p-1f,0x1.782fb20000000p-1f,0x1.5b50b20000000p-1f,0x1.7940060000000p-1f,0x1.5a28d20000000p-1f,0x1.7a4f700000000p-1f,0x1.59001e0000000p-1f,
0x1.7b5df20000000p-1f,0x1.57d6940000000p-1f,0x1.7c6b8a0000000p-1f,0x1.56ac360000000p-1f,0x1.7d78360000000p-1f,0x1.5581040000000p-1f,0x1.7e83f80000000p-1f,0x1.5455000000000p-1f,
0x1.7f8ece0000000p-1f,0x1.53282a0000000p-1f,0x1.8098b80000000p-1f,0x1.51fa820000000p-1f,0x1.81a1b40000000p-1f,0x1.50cc0a0000000p-1f,0x1.82a9c20000000p-1f,0x1.4f9cc20000000p-1f,
0x1.83b0e00000000p-1f,0x1.4e6cac0000000p-1f,0x1.84b7120000000p-1f,0x1.4d3bc60000000p-1f,0x1.85bc520000000p-1f,0x1.4c0a140000000p-1f,0x1.86c0a20000000p-1f,0x1.4ad7960000000p-1f,
0x1.87c4000000000p-1f,0x1.49a44a0000000p-1f,0x1.88c66e0000000p-1f,0x1.4870340000000p-1f,0x1.89c7ea0000000p-1f,0x1.473b520000000p-1f,0x1.8ac8720000000p-1f,0x1.4605a60000000p-1f,
0x1.8bc8060000000p-1f,0x1.44cf320000000p-1f,0x1.8cc6a80000000p-1f,0x1.4397f60000000p-1f,0x1.8dc4540000000p-1f,0x1.425ff20000000p-1f,0x1.8ec10a0000000p-1f,0x1.4127260000000p-1f,
0x1.8fbcca0000000p-1f,0x1.3fed960000000p-1f,0x1.90b7940000000p-1f,0x1.3eb33e0000000p-1f,0x1.91b1660000000p-1f,0x1.3d78240000000p-1f,0x1.92aa420000000p-1f,0x1.3c3c440000000p-1f,
0x1.93a2240000000p-1f,0x1.3affa20000000p-1f,0x1.94990e0000000p-1f,0x1.39c23e0000000p-1f,0x1.958efe0000000p-1f,0x1.3884180000000p-1f,0x1.9683f40000000p-1f,0x1.3745320000000p-1f,
0x1.9777f00000000p-1f,0x1.36058c0000000p-1f,0x1.986af00000000p-1f,0x1.34c5260000000p-1f,0x1.995cf20000000p-1f,0x1.3384000000000p-1f,0x1.9a4dfa0000000p-1f,0x1.32421e0000000p-1f,
0x1.9b3e040000000p-1f,0x1.30ff800000000p-1f,0x1.9c2d120000000p-1f,0x1.2fbc240000000p-1f,0x1.9d1b200000000p-1f,0x1.2e780e0000000p-1f,0x1.9e082e0000000p-1f,0x1.2d333e0000000p-1f,
0x1.9ef43e0000000p-1f,0x1.2bedb20000000p-1f,0x1.9fdf500000000p-1f,0x1.2aa76e0000000p-1f,0x1.a0c95e0000000p-1f,0x1.2960720000000p-1f,0x1.a1b26e0000000p-1f,0x1.2818be0000000p-1f,
0x1.a29a7a0000000p-1f,0x1.26d0540000000p-1f,0x1.a381840000000p-1f,0x1.2587340000000p-1f,0x1.a4678c0000000p-1f,0x1.243d600000000p-1f,0x1.a54c920000000p-1f,0x1.22f2d60000000p-1f,
0x1.a630920000000p-1f,0x1.21a79a0000000p-1f,0x1.a7138e0000000p-1f,0x1.205baa0000000p-1f,0x1.a7f5860000000p-1f,0x1.1f0f080000000p-1f,0x1.a8d6760000000p-1f,0x1.1dc1b60000000p-1f,
0x1.a9b6620000000p-1f,0x1.1c73b40000000p-1f,0x1.aa95480000000p-1f,0x1.1b25020000000p-1f,0x1.ab73260000000p-1f,0x1.19d5a00000000p-1f,0x1.ac4ffc0000000p-1f,0x1.1885920000000p-1f,
0x1.ad2bca0000000p-1f,0x1.1734d60000000p-1f,0x1.ae06900000000p-1f,0x1.15e36e0000000p-1f,0x1.aee04c0000000p-1f,0x1.14915a0000000p-1f,0x1.afb8fe0000000p-1f,0x1.133e9c0000000p-1f,
0x1.b090a60000000p-1f,0x1.11eb360000000p-1f,0x1.b167420000000p-1f,0x1.1097240000000p-1f,0x1.b23cd40000000p-1f,0x1.0f426c0000000p-1f,0x1.b3115a0000000p-1f,0x1.0ded0c0000000p-1f,
0x1.b3e4d40000000p-1f,0x1.0c97040000000p-1f,0x1.b4b7400000000p-1f,0x1.0b40580000000p-1f,0x1.b588a00000000p-1f,0x1.09e9080000000p-1f,0x1.b658f20000000p-1f,0x1.0891120000000p-1f,
0x1.b728340000000p-1f,0x1.07387a0000000p-1f,0x1.b7f6680000000p-1f,0x1.05df3e0000000p-1f,0x1.b8c38e0000000p-1f,0x1.0485620000000p-1f,0x1.b98fa20000000p-1f,0x1.032ae60000000p-1f,
0x1.ba5aa60000000p-1f,0x1.01cfc80000000p-1f,0x1.bb249a0000000p-1f,0x1.00740c0000000p-1f,0x1.bbed7c0000000p-1f,0x1.fe2f640000000p-2f,0x1.bcb54c0000000p-1f,0x1.fb75760000000p-2f,
0x1.bd7c0a0000000p-1f,0x1.f8ba4e0000000p-2f,0x1.be41b60000000p-1f,0x1.f5fdee0000000p-2f,0x1.bf064e0000000p-1f,0x1.f3405a0000000p-2f,0x1.bfc9d20000000p-1f,0x1.f081900000000p-2f,
0x1.c08c420000000p-1f,0x1.edc1960000000p-2f,0x1.c14d9e0000000p-1f,0x1.eb006a0000000p-2f,0x1.c20de40000000p-1f,0x1.e83e0e0000000p-2f,0x1.c2cd140000000p-1f,0x1.e57a860000000p-2f,
0x1.c38b300000000p-1f,0x1.e2b5d40000000p-2f,0x1.c448340000000p-1f,0x1.dfeff60000000p-2f,0x1.c504200000000p-1f,0x1.dd28f20000000p-2f,0x1.c5bef60000000p-1f,0x1.da60c60000000p-2f,
0x1.c678b40000000p-1f,0x1.d797760000000p-2f,0x1.c731580000000p-1f,0x1.d4cd020000000p-2f,0x1.c7e8e60000000p-1f,0x1.d2016e0000000p-2f,0x1.c89f580000000p-1f,0x1.cf34ba0000000p-2f,
0x1.c954b20000000p-1f,0x1.cc66ea0000000p-2f,0x1.ca08f20000000p-1f,0x1.c997fc0000000p-2f,0x1.cabc160000000p-1f,0x1.c6c7f40000000p-2f,0x1.cb6e200000000p-1f,0x1.c3f6d40000000p-2f,
0x1.cc1f100000000p-1f,0x1.c1249e0000000p-2f,0x1.cccee20000000p-1f,0x1.be51520000000p-2f,0x1.cd7d980000000p-1f,0x1.bb7cf20000000p-2f,0x1.ce2b320000000p-1f,0x1.b8a7820000000p-2f,
0x1.ced7b00000000p-1f,0x1.b5d1000000000p-2f,0x1.cf830e0000000p-1f,0x1.b2f9720000000p-2f,0x1.d02d500000000p-1f,0x1.b020d60000000p-2f,0x1.d0d6720000000p-1f,0x1.ad47320000000p-2f,
0x1.d17e780000000p-1f,0x1.aa6c820000000p-2f,0x1.d2255c0000000p-1f,0x1.a790ce0000000p-2f,0x1.d2cb220000000p-1f,0x1.a4b4120000000p-2f,0x1.d36fc80000000p-1f,0x1.a1d6540000000p-2f,
0x1.d4134e0000000p-1f,0x1.9ef7940000000p-2f,0x1.d4b5b20000000p-1f,0x1.9c17d40000000p-2f,0x1.d556f60000000p-1f,0x1.9937160000000p-2f,0x1.d5f7180000000p-1f,0x1.96555c0000000p-2f,
0x1.d696180000000p-1f,0x1.9372a60000000p-2f,0x1.d733f60000000p-1f,0x1.908ef80000000p-2f,0x1.d7d0b00000000p-1f,0x1.8daa520000000p-2f,0x1.d86c480000000p-1f,0x1.8ac4b80000000p-2f,
0x1.d906bc0000000p-1f,0x1.87de2a0000000p-2f,0x1.d9a00e0000000p-1f,0x1.84f6aa0000000p-2f,0x1.da383a0000000p-1f,0x1.820e3c0000000p-2f,0x1.dacf420000000p-1f,0x1.7f24de0000000p-2f,
0x1.db65260000000p-1f,0x1.7c3a940000000p-2f,0x1.dbf9e40000000p-1f,0x1.794f5e0000000p-2f,0x1.dc8d7c0000000p-1f,0x1.7663400000000p-2f,0x1.dd1ff00000000p-1f,0x1.73763c0000000p-2f,
0x1.ddb13c0000000p-1f,0x1.7088540000000p-2f,0x1.de41600000000p-1f,0x1.6d99860000000p-2f,0x1.ded0600000000p-1f,0x1.6aa9d80000000p-2f,0x1.df5e360000000p-1f,0x1.67b94a0000000p-2f,
0x1.dfeae60000000p-1f,0x1.64c7de0000000p-2f,0x1.e0766e0000000p-1f,0x1.61d5960000000p-2f,0x1.e100cc0000000p-1f,0x1.5ee2740000000p-2f,0x1.e18a020000000p-1f,0x1.5bee780000000p-2f,
0x1.e212100000000p-1f,0x1.58f9a80000000p-2f,0x1.e298f40000000p-1f,0x1.5604020000000p-2f,0x1.e31eae0000000p-1f,0x1.530d880000000p-2f,0x1.e3a33e0000000p-1f,0x1.50163e0000000p-2f,
0x1.e426a40000000p-1f,0x1.4d1e240000000p-2f,0x1.e4a8e00000000p-1f,0x1.4a253e0000000p-2f,0x1.e529f00000000p-1f,0x1.472b8a0000000p-2f,0x1.e5a9d60000000p-1f,0x1.44310e0000000p-2f,
0x1.e6288e0000000p-1f,0x1.4135ca0000000p-2f,0x1.e6a61c0000000p-1f,0x1.3e39be0000000p-2f,0x1.e7227e0000000p-1f,0x1.3b3cf00000000p-2f,0x1.e79db20000000p-1f,0x1.383f5e0000000p-2f,
0x1.e817ba0000000p-1f,0x1.35410c0000000p-2f,0x1.e890960000000p-1f,0x1.3241fc0000000p-2f,0x1.e908440000000p-1f,0x1.2f422e0000000p-2f,0x1.e97ec40000000p-1f,0x1.2c41a40000000p-2f,
0x1.e9f4160000000p-1f,0x1.2940620000000p-2f,0x1.ea683a0000000p-1f,0x1.263e6a0000000p-2f,0x1.eadb2e0000000p-1f,0x1.233bba0000000p-2f,0x1.eb4cf60000000p-1f,0x1.2038580000000p-2f,
0x1.ebbd8c0000000p-1f,0x1.1d34440000000p-2f,0x1.ec2cf40000000p-1f,0x1.1a2f800000000p-2f,0x1.ec9b2e0000000p-1f,0x1.172a0e0000000p-2f,0x1.ed08360000000p-1f,0x1.1423ee0000000p-2f,
0x1.ed740e0000000p-1f,0x1.111d260000000p-2f,0x1.eddeb60000000p-1f,0x1.0e15b40000000p-2f,0x1.ee482e0000000p-1f,0x1.0b0d9c0000000p-2f,0x1.eeb0740000000p-1f,0x1.0804e00000000p-2f,
0x1.ef178a0000000p-1f,0x1.04fb800000000p-2f,0x1.ef7d6e0000000p-1f,0x1.01f1800000000p-2f,0x1.efe2200000000p-1f,0x1.fdcdc20000000p-3f,0x1.f045a20000000p-1f,0x1.f7b7480000000p-3f,
0x1.f0a7f00000000p-1f,0x1.f19f980000000p-3f,0x1.f1090c0000000p-1f,0x1.eb86b40000000p-3f,0x1.f168f60000000p-1f,0x1.e56ca20000000p-3f,0x1.f1c7ac0000000p-1f,0x1.df51640000000p-3f,
0x1.f225300000000p-1f,0x1.d934fe0000000p-3f,0x1.f281800000000p-1f,0x1.d317740000000p-3f,0x1.f2dc9c0000000p-1f,0x1.ccf8cc0000000p-3f,0x1.f336860000000p-1f,0x1.c6d9060000000p-3f,
0x1.f38f3a0000000p-1f,0x1.c0b8260000000p-3f,0x1.f3e6bc0000000p-1f,0x1.ba96340000000p-3f,0x1.f43d080000000p-1f,0x1.b4732e0000000p-3f,0x1.f492200000000p-1f,0x1.ae4f1e0000000p-3f,
0x1.f4e6040000000p-1f,0x1.a82a020000000p-3f,0x1.f538b20000000p-1f,0x1.a203e20000000p-3f,0x1.f58a2c0000000p-1f,0x1.9bdcc00000000p-3f,0x1.f5da6e0000000p-1f,0x1.95b49e0000000p-3f,
0x1.f6297c0000000p-1f,0x1.8f8b840000000p-3f,0x1.f677560000000p-1f,0x1.8961720000000p-3f,0x1.f6c3f80000000p-1f,0x1.83366e0000000p-3f,0x1.f70f640000000p-1f,0x1.7d0a7c0000000p-3f,
0x1.f7599a0000000p-1f,0x1.76dd9e0000000p-3f,0x1.f7a29a0000000p-1f,0x1.70afd80000000p-3f,0x1.f7ea620000000p-1f,0x1.6a81300000000p-3f,0x1.f830f40000000p-1f,0x1.6451a80000000p-3f,
0x1.f876500000000p-1f,0x1.5e21440000000p-3f,0x1.f8ba740000000p-1f,0x1.57f0080000000p-3f,0x1.f8fd600000000p-1f,0x1.51bdf80000000p-3f,0x1.f93f140000000p-1f,0x1.4b8b180000000p-3f,
0x1.f97f920000000p-1f,0x1.45576c0000000p-3f,0x1.f9bed80000000p-1f,0x1.3f22f60000000p-3f,0x1.f9fce60000000p-1f,0x1.38edbc0000000p-3f,0x1.fa39ba0000000p-1f,0x1.32b7c00000000p-3f,
0x1.fa75580000000p-1f,0x1.2c81060000000p-3f,0x1.faafbc0000000p-1f,0x1.2649940000000p-3f,0x1.fae8e80000000p-1f,0x1.20116e0000000p-3f,0x1.fb20dc0000000p-1f,0x1.19d8940000000p-3f,
0x1.fb57980000000p-1f,0x1.139f0c0000000p-3f,0x1.fb8d180000000p-1f,0x1.0d64dc0000000p-3f,0x1.fbc1620000000p-1f,0x1.072a040000000p-3f,0x1.fbf4700000000p-1f,0x1.00ee8a0000000p-3f,
0x1.fc26480000000p-1f,0x1.f564e60000000p-4f,0x1.fc56e40000000p-1f,0x1.e8eb800000000p-4f,0x1.fc86460000000p-1f,0x1.dc70ec0000000p-4f,0x1.fcb4700000000p-1f,0x1.cff5340000000p-4f,
0x1.fce1600000000p-1f,0x1.c3785c0000000p-4f,0x1.fd0d160000000p-1f,0x1.b6fa6e0000000p-4f,0x1.fd37920000000p-1f,0x1.aa7b720000000p-4f,0x1.fd60d20000000p-1f,0x1.9dfb6e0000000p-4f,
0x1.fd88da0000000p-1f,0x1.917a6c0000000p-4f,0x1.fdafa80000000p-1f,0x1.84f8720000000p-4f,0x1.fdd53a0000000p-1f,0x1.7875860000000p-4f,0x1.fdf9920000000p-1f,0x1.6bf1b40000000p-4f,
0x1.fe1cb00000000p-1f,0x1.5f6d000000000p-4f,0x1.fe3e920000000p-1f,0x1.52e7740000000p-4f,0x1.fe5f3a0000000p-1f,0x1.4661180000000p-4f,0x1.fe7ea80000000p-1f,0x1.39d9f20000000p-4f,
0x1.fe9cda0000000p-1f,0x1.2d520a0000000p-4f,0x1.feb9d20000000p-1f,0x1.20c9680000000p-4f,0x1.fed58e0000000p-1f,0x1.1440140000000p-4f,0x1.fef0100000000p-1f,0x1.07b6140000000p-4f,
0x1.ff09560000000p-1f,0x1.f656e80000000p-5f,0x1.ff21620000000p-1f,0x1.dd40700000000p-5f,0x1.ff38300000000p-1f,0x1.c428d20000000p-5f,0x1.ff4dc60000000p-1f,0x1.ab101c0000000p-5f,
0x1.ff621e0000000p-1f,0x1.91f6600000000p-5f,0x1.ff753c0000000p-1f,0x1.78dbaa0000000p-5f,0x1.ff871e0000000p-1f,0x1.5fc00e0000000p-5f,0x1.ff97c40000000p-1f,0x1.46a3960000000p-5f,
0x1.ffa72e0000000p-1f,0x1.2d86580000000p-5f,0x1.ffb55e0000000p-1f,0x1.14685e0000000p-5f,0x1.ffc2520000000p-1f,0x1.f693740000000p-6f,0x1.ffce0a0000000p-1f,0x1.c454f40000000p-6f,
0x1.ffd8860000000p-1f,0x1.9215600000000p-6f,0x1.ffe1c60000000p-1f,0x1.5fd4d20000000p-6f,0x1.ffe9cc0000000p-1f,0x1.2d936c0000000p-6f,0x1.fff0940000000p-1f,0x1.f6a2960000000p-7f,
0x1.fff6220000000p-1f,0x1.921d200000000p-7f,0x1.fffa720000000p-1f,0x1.2d96b00000000p-7f,0x1.fffd880000000p-1f,0x1.921f100000000p-8f,0x1.ffff620000000p-1f,0x1.921f8c0000000p-9f,
};
__device__ const float TW448[896] = {
0x1.0000000000000p+0f,0.0f,0x1.fff31c0000000p-1f,-0x1.cb8e180000000p-7f,0x1.ffcc700000000p-1f,-0x1.cb82860000000p-6f,0x1.ff8c000000000p-1f,-0x1.58936e0000000p-5f,
0x1.ff31cc0000000p-1f,-0x1.cb54400000000p-5f,0x1.febddc0000000p-1f,-0x1.1efef80000000p-4f,0x1.fe30340000000p-1f,-0x1.58455c0000000p-4f,0x1.fd88da0000000p-1f,-0x1.917a6c0000000p-4f,
0x1.fcc7d80000000p-1f,-0x1.ca9b440000000p-4f,0x1.fbed380000000p-1f,-0x1.01d2820000000p-3f,0x1.faf9060000000p-1f,-0x1.1e4a660000000p-3f,0x1.f9eb4a0000000p-1f,-0x1.3ab3e00000000p-3f,
0x1.f8c4160000000p-1f,-0x1.570d800000000p-3f,0x1.f783760000000p-1f,-0x1.7355dc0000000p-3f,0x1.f6297c0000000p-1f,-0x1.8f8b840000000p-3f,0x1.f4b63a0000000p-1f,-0x1.abad0e0000000p-3f,
0x1.f329c00000000p-1f,-0x1.c7b90e0000000p-3f,0x1.f184240000000p-1f,-0x1.e3ae1c0000000p-3f,0x1.efc57a0000000p-1f,-0x1.ff8ad00000000p-3f,0x1.ededda0000000p-1f,-0x1.0da6e00000000p-2f,
0x1.ebfd5a0000000p-1f,-0x1.1b7ac40000000p-2f,0x1.e9f4160000000p-1f,-0x1.2940620000000p-2f,0x1.e7d2240000000p-1f,-0x1.36f70a0000000p-2f,0x1.e597a20000000p-1f,-0x1.449e080000000p-2f,
0x1.e344ae0000000p-1f,-0x1.5234ac0000000p-2f,0x1.e0d9620000000p-1f,-0x1.5fba4a0000000p-2f,0x1.de55e00000000p-1f,-0x1.6d2e320000000p-2f,0x1.dbba480000000p-1f,-0x1.7a8fb60000000p-2f,
0x1.d906bc0000000p-1f,-0x1.87de2a0000000p-2f,0x1.d63b600000000p-1f,-0x1.9518e40000000p-2f,0x1.d358540000000p-1f,-0x1.a23f360000000p-2f,0x1.d05dc00000000p-1f,-0x1.af507a0000000p-2f,
0x1.cd4bca0000000p-1f,-0x1.bc4c040000000p-2f,0x1.ca229a0000000p-1f,-0x1.c931300000000p-2f,0x1.c6e2580000000p-1f,-0x1.d5ff580000000p-2f,0x1.c38b300000000p-1f,-0x1.e2b5d40000000p-2f,
0x1.c01d480000000p-1f,-0x1.ef54020000000p-2f,0x1.bc98d20000000p-1f,-0x1.fbd93e0000000p-2f,0x1.b8fdf80000000p-1f,-0x1.0422740000000p-1f,0x1.b54cea0000000p-1f,-0x1.0a4b300000000p-1f,
0x1.b185d60000000p-1f,-0x1.1066820000000p-1f,0x1.ada8ee0000000p-1f,-0x1.16741e0000000p-1f,0x1.a9b6620000000p-1f,-0x1.1c73b40000000p-1f,0x1.a5ae680000000p-1f,-0x1.2264f60000000p-1f,
0x1.a191320000000p-1f,-0x1.28479a0000000p-1f,0x1.9d5ef40000000p-1f,-0x1.2e1b540000000p-1f,0x1.9917e60000000p-1f,-0x1.33dfd60000000p-1f,0x1.94bc400000000p-1f,-0x1.3994d60000000p-1f,
0x1.904c380000000p-1f,-0x1.3f3a0e0000000p-1f,0x1.8bc8060000000p-1f,-0x1.44cf320000000p-1f,0x1.872fe80000000p-1f,-0x1.4a53fc0000000p-1f,0x1.8284160000000p-1f,-0x1.4fc8220000000p-1f,
0x1.7dc4d00000000p-1f,-0x1.552b600000000p-1f,0x1.78f24e0000000p-1f,-0x1.5a7d720000000p-1f,0x1.740cd20000000p-1f,-0x1.5fbe100000000p-1f,0x1.6f149a0000000p-1f,-0x1.64ecf80000000p-1f,
0x1.6a09e60000000p-1f,-0x1.6a09e60000000p-1f,0x1.64ecf80000000p-1f,-0x1.6f149a0000000p-1f,0x1.5fbe100000000p-1f,-0x1.740cd20000000p-1f,0x1.5a7d720000000p-1f,-0x1.78f24e0000000p-1f,
0x1.552b600000000p-1f,-0x1.7dc4d00000000p-1f,0x1.4fc8220000000p-1f,-0x1.8284160000000p-1f,0x1.4a53fc0000000p-1f,-0x1.872fe80000000p-1f,0x1.44cf320000000p-1f,-0x1.8bc8060000000p-1f,
0x1.3f3a0e0000000p-1f,-0x1.904c380000000p-1f,0x1.3994d60000000p-1f,-0x1.94bc400000000p-1f,0x1.33dfd60000000p-1f,-0x1.9917e60000000p-1f,0x1.2e1b540000000p-1f,-0x1.9d5ef40000000p-1f,
0x1.28479a0000000p-1f,-0x1.a191320000000p-1f,0x1.2264f60000000p-1f,-0x1.a5ae680000000p-1f,0x1.1c73b40000000p-1f,-0x1.a9b6620000000p-1f,0x1.16741e0000000p-1f,-0x1.ada8ee0000000p-1f,
0x1.1066820000000p-1f,-0x1.b185d60000000p-1f,0x1.0a4b300000000p-1f,-0x1.b54cea0000000p-1f,0x1.0422740000000p-1f,-0x1.b8fdf80000000p-1f,0x1.fbd93e0000000p-2f,-0x1.bc98d20000000p-1f,
0x1.ef54020000000p-2f,-0x1.c01d480000000p-1f,0x1.e2b5d40000000p-2f,-0x1.c38b300000000p-1f,0x1.d5ff580000000p-2f,-0x1.c6e2580000000p-1f,0x1.c931300000000p-2f,-0x1.ca229a0000000p-1f,
0x1.bc4c040000000p-2f,-0x1.cd4bca0000000p-1f,0x1.af507a0000000p-2f,-0x1.d05dc00000000p-1f,0x1.a23f360000000p-2f,-0x1.d358540000000p-1f,0x1.9518e40000000p-2f,-0x1.d63b600000000p-1f,
0x1.87de2a0000000p-2f,-0x1.d906bc0000000p-1f,0x1.7a8fb60000000p-2f,-0x1.dbba480000000p-1f,0x1.6d2e320000000p-2f,-0x1.de55e00000000p-1f,0x1.5fba4a0000000p-2f,-0x1.e0d9620000000p-1f,
0x1.5234ac0000000p-2f,-0x1.e344ae0000000p-1f,0x1.449e080000000p-2f,-0x1.e597a20000000p-1f,0x1.36f70a0000000p-2f,-0x1.e7d2240000000p-1f,0x1.2940620000000p-2f,-0x1.e9f4160000000p-1f,
0x1.1b7ac40000000p-2f,-0x1.ebfd5a0000000p-1f,0x1.0da6e00000000p-2f,-0x1.ededda0000000p-1f,0x1.ff8ad00000000p-3f,-0x1.efc57a0000000p-1f,0x1.e3ae1c0000000p-3f,-0x1.f184240000000p-1f,
0x1.c7b90e0000000p-3f,-0x1.f329c00000000p-1f,0x1.abad0e0000000p-3f,-0x1.f4b63a0000000p-1f,0x1.8f8b840000000p-3f,-0x1.f6297c0000000p-1f,0x1.7355dc0000000p-3f,-0x1.f783760000000p-1f,
0x1.570d800000000p-3f,-0x1.f8c4160000000p-1f,0x1.3ab3e00000000p-3f,-0x1.f9eb4a0000000p-1f,0x1.1e4a660000000p-3f,-0x1.faf9060000000p-1f,0x1.01d2820000000p-3f,-0x1.fbed380000000p-1f,
0x1.ca9b440000000p-4f,-0x1.fcc7d80000000p-1f,0x1.917a6c0000000p-4f,-0x1.fd88da0000000p-1f,0x1.58455c0000000p-4f,-0x1.fe30340000000p-1f,0x1.1efef80000000p-4f,-0x1.febddc0000000p-1f,
0x1.cb54400000000p-5f,-0x1.ff31cc0000000p-1f,0x1.58936e0000000p-5f,-0x1.ff8c000000000p-1f,0x1.cb82860000000p-6f,-0x1.ffcc700000000p-1f,0x1.cb8e180000000p-7f,-0x1.fff31c0000000p-1f,
0.0f,-0x1.0000000000000p+0f,-0x1.cb8e180000000p-7f,-0x1.fff31c0000000p-1f,-0x1.cb82860000000p-6f,-0x1.ffcc700000000p-1f,-0x1.58936e0000000p-5f,-0x1.ff8c000000000p-1f,
-0x1.cb54400000000p-5f,-0x1.ff31cc0000000p-1f,-0x1.1efef80000000p-4f,-0x1.febddc0000000p-1f,-0x1.58455c0000000p-4f,-0x1.fe30340000000p-1f,-0x1.917a6c0000000p-4f,-0x1.fd88da0000000p-1f,
-0x1.ca9b440000000p-4f,-0x1.fcc7d80000000p-1f,-0x1.01d2820000000p-3f,-0x1.fbed380000000p-1f,-0x1.1e4a660000000p-3f,-0x1.faf9060000000p-1f,-0x1.3ab3e00000000p-3f,-0x1.f9eb4a0000000p-1f,
-0x1.570d800000000p-3f,-0x1.f8c4160000000p-1f,-0x1.7355dc0000000p-3f,-0x1.f783760000000p-1f,-0x1.8f8b840000000p-3f,-0x1.f6297c0000000p-1f,-0x1.abad0e0000000p-3f,-0x1.f4b63a0000000p-1f,
-0x1.c7b90e0000000p-3f,-0x1.f329c00000000p-1f,-0x1.e3ae1c0000000p-3f,-0x1.f184240000000p-1f,-0x1.ff8ad00000000p-3f,-0x1.efc57a0000000p-1f,-0x1.0da6e00000000p-2f,-0x1.ededda0000000p-1f,
-0x1.1b7ac40000000p-2f,-0x1.ebfd5a0000000p-1f,-0x1.2940620000000p-2f,-0x1.e9f4160000000p-1f,-0x1.36f70a0000000p-2f,-0x1.e7d2240000000p-1f,-0x1.449e080000000p-2f,-0x1.e597a20000000p-1f,
-0x1.5234ac0000000p-2f,-0x1.e344ae0000000p-1f,-0x1.5fba4a0000000p-2f,-0x1.e0d9620000000p-1f,-0x1.6d2e320000000p-2f,-0x1.de55e00000000p-1f,-0x1.7a8fb60000000p-2f,-0x1.dbba480000000p-1f,
-0x1.87de2a0000000p-2f,-0x1.d906bc0000000p-1f,-0x1.9518e40000000p-2f,-0x1.d63b600000000p-1f,-0x1.a23f360000000p-2f,-0x1.d358540000000p-1f,-0x1.af507a0000000p-2f,-0x1.d05dc00000000p-1f,
-0x1.bc4c040000000p-2f,-0x1.cd4bca0000000p-1f,-0x1.c931300000000p-2f,-0x1.ca229a0000000p-1f,-0x1.d5ff580000000p-2f,-0x1.c6e2580000000p-1f,-0x1.e2b5d40000000p-2f,-0x1.c38b300000000p-1f,
-0x1.ef54020000000p-2f,-0x1.c01d480000000p-1f,-0x1.fbd93e0000000p-2f,-0x1.bc98d20000000p-1f,-0x1.0422740000000p-1f,-0x1.b8fdf80000000p-1f,-0x1.0a4b300000000p-1f,-0x1.b54cea0000000p-1f,
-0x1.1066820000000p-1f,-0x1.b185d60000000p-1f,-0x1.16741e0000000p-1f,-0x1.ada8ee0000000p-1f,-0x1.1c73b40000000p-1f,-0x1.a9b6620000000p-1f,-0x1.2264f60000000p-1f,-0x1.a5ae680000000p-1f,
-0x1.28479a0000000p-1f,-0x1.a191320000000p-1f,-0x1.2e1b540000000p-1f,-0x1.9d5ef40000000p-1f,-0x1.33dfd60000000p-1f,-0x1.9917e60000000p-1f,-0x1.3994d60000000p-1f,-0x1.94bc400000000p-1f,
-0x1.3f3a0e0000000p-1f,-0x1.904c380000000p-1f,-0x1.44cf320000000p-1f,-0x1.8bc8060000000p-1f,-0x1.4a53fc0000000p-1f,-0x1.872fe80000000p-1f,-0x1.4fc8220000000p-1f,-0x1.8284160000000p-1f,
-0x1.552b600000000p-1f,-0x1.7dc4d00000000p-1f,-0x1.5a7d720000000p-1f,-0x1.78f24e0000000p-1f,-0x1.5fbe100000000p-1f,-0x1.740cd20000000p-1f,-0x1.64ecf80000000p-1f,-0x1.6f149a0000000p-1f,
-0x1.6a09e60000000p-1f,-0x1.6a09e60000000p-1f,-0x1.6f149a0000000p-1f,-0x1.64ecf80000000p-1f,-0x1.740cd20000000p-1f,-0x1.5fbe100000000p-1f,-0x1.78f24e0000000p-1f,-0x1.5a7d720000000p-1f,
-0x1.7dc4d00000000p-1f,-0x1.552b600000000p-1f,-0x1.8284160000000p-1f,-0x1.4fc8220000000p-1f,-0x1.872fe80000000p-1f,-0x1.4a53fc0000000p-1f,-0x1.8bc8060000000p-1f,-0x1.44cf320000000p-1f,
-0x1.904c380000000p-1f,-0x1.3f3a0e0000000p-1f,-0x1.94bc400000000p-1f,-0x1.3994d60000000p-1f,-0x1.9917e60000000p-1f,-0x1.33dfd60000000p-1f,-0x1.9d5ef40000000p-1f,-0x1.2e1b540000000p-1f,
-0x1.a191320000000p-1f,-0x1.28479a0000000p-1f,-0x1.a5ae680000000p-1f,-0x1.2264f60000000p-1f,-0x1.a9b6620000000p-1f,-0x1.1c73b40000000p-1f,-0x1.ada8ee0000000p-1f,-0x1.16741e0000000p-1f,
-0x1.b185d60000000p-1f,-0x1.1066820000000p-1f,-0x1.b54cea0000000p-1f,-0x1.0a4b300000000p-1f,-0x1.b8fdf80000000p-1f,-0x1.0422740000000p-1f,-0x1.bc98d20000000p-1f,-0x1.fbd93e0000000p-2f,
-0x1.c01d480000000p-1f,-0x1.ef54020000000p-2f,-0x1.c38b300000000p-1f,-0x1.e2b5d40000000p-2f,-0x1.c6e2580000000p-1f,-0x1.d5ff580000000p-2f,-0x1.ca229a0000000p-1f,-0x1.c931300000000p-2f,
-0x1.cd4bca0000000p-1f,-0x1.bc4c040000000p-2f,-0x1.d05dc00000000p-1f,-0x1.af507a0000000p-2f,-0x1.d358540000000p-1f,-0x1.a23f360000000p-2f,-0x1.d63b600000000p-1f,-0x1.9518e40000000p-2f,
-0x1.d906bc0000000p-1f,-0x1.87de2a0000000p-2f,-0x1.dbba480000000p-1f,-0x1.7a8fb60000000p-2f,-0x1.de55e00000000p-1f,-0x1.6d2e320000000p-2f,-0x1.e0d9620000000p-1f,-0x1.5fba4a0000000p-2f,
-0x1.e344ae0000000p-1f,-0x1.5234ac0000000p-2f,-0x1.e597a20000000p-1f,-0x1.449e080000000p-2f,-0x1.e7d2240000000p-1f,-0x1.36f70a0000000p-2f,-0x1.e9f4160000000p-1f,-0x1.2940620000000p-2f,
-0x1.ebfd5a0000000p-1f,-0x1.1b7ac40000000p-2f,-0x1.ededda0000000p-1f,-0x1.0da6e00000000p-2f,-0x1.efc57a0000000p-1f,-0x1.ff8ad00000000p-3f,-0x1.f184240000000p-1f,-0x1.e3ae1c0000000p-3f,
-0x1.f329c00000000p-1f,-0x1.c7b90e0000000p-3f,-0x1.f4b63a0000000p-1f,-0x1.abad0e0000000p-3f,-0x1.f6297c0000000p-1f,-0x1.8f8b840000000p-3f,-0x1.f783760000000p-1f,-0x1.7355dc0000000p-3f,
-0x1.f8c4160000000p-1f,-0x1.570d800000000p-3f,-0x1.f9eb4a0000000p-1f,-0x1.3ab3e00000000p-3f,-0x1.faf9060000000p-1f,-0x1.1e4a660000000p-3f,-0x1.fbed380000000p-1f,-0x1.01d2820000000p-3f,
-0x1.fcc7d80000000p-1f,-0x1.ca9b440000000p-4f,-0x1.fd88da0000000p-1f,-0x1.917a6c0000000p-4f,-0x1.fe30340000000p-1f,-0x1.58455c0000000p-4f,-0x1.febddc0000000p-1f,-0x1.1efef80000000p-4f,
-0x1.ff31cc0000000p-1f,-0x1.cb54400000000p-5f,-0x1.ff8c000000000p-1f,-0x1.58936e0000000p-5f,-0x1.ffcc700000000p-1f,-0x1.cb82860000000p-6f,-0x1.fff31c0000000p-1f,-0x1.cb8e180000000p-7f,
-0x1.0000000000000p+0f,0.0f,-0x1.fff31c0000000p-1f,0x1.cb8e180000000p-7f,-0x1.ffcc700000000p-1f,0x1.cb82860000000p-6f,-0x1.ff8c000000000p-1f,0x1.58936e0000000p-5f,
-0x1.ff31cc0000000p-1f,0x1.cb54400000000p-5f,-0x1.febddc0000000p-1f,0x1.1efef80000000p-4f,-0x1.fe30340000000p-1f,0x1.58455c0000000p-4f,-0x1.fd88da0000000p-1f,0x1.917a6c0000000p-4f,
-0x1.fcc7d80000000p-1f,0x1.ca9b440000000p-4f,-0x1.fbed380000000p-1f,0x1.01d2820000000p-3f,-0x1.faf9060000000p-1f,0x1.1e4a660000000p-3f,-0x1.f9eb4a0000000p-1f,0x1.3ab3e00000000p-3f,
-0x1.f8c4160000000p-1f,0x1.570d800000000p-3f,-0x1.f783760000000p-1f,0x1.7355dc0000000p-3f,-0x1.f6297c0000000p-1f,0x1.8f8b840000000p-3f,-0x1.f4b63a0000000p-1f,0x1.abad0e0000000p-3f,
-0x1.f329c00000000p-1f,0x1.c7b90e0000000p-3f,-0x1.f184240000000p-1f,0x1.e3ae1c0000000p-3f,-0x1.efc57a0000000p-1f,0x1.ff8ad00000000p-3f,-0x1.ededda0000000p-1f,0x1.0da6e00000000p-2f,
-0x1.ebfd5a0000000p-1f,0x1.1b7ac40000000p-2f,-0x1.e9f4160000000p-1f,0x1.2940620000000p-2f,-0x1.e7d2240000000p-1f,0x1.36f70a0000000p-2f,-0x1.e597a20000000p-1f,0x1.449e080000000p-2f,
-0x1.e344ae0000000p-1f,0x1.5234ac0000000p-2f,-0x1.e0d9620000000p-1f,0x1.5fba4a0000000p-2f,-0x1.de55e00000000p-1f,0x1.6d2e320000000p-2f,-0x1.dbba480000000p-1f,0x1.7a8fb60000000p-2f,
-0x1.d906bc0000000p-1f,0x1.87de2a0000000p-2f,-0x1.d63b600000000p-1f,0x1.9518e40000000p-2f,-0x1.d358540000000p-1f,0x1.a23f360000000p-2f,-0x1.d05dc00000000p-1f,0x1.af507a0000000p-2f,
-0x1.cd4bca0000000p-1f,0x1.bc4c040000000p-2f,-0x1.ca229a0000000p-1f,0x1.c931300000000p-2f,-0x1.c6e2580000000p-1f,0x1.d5ff580000000p-2f,-0x1.c38b300000000p-1f,0x1.e2b5d40000000p-2f,
-0x1.c01d480000000p-1f,0x1.ef54020000000p-2f,-0x1.bc98d20000000p-1f,0x1.fbd93e0000000p-2f,-0x1.b8fdf80000000p-1f,0x1.0422740000000p-1f,-0x1.b54cea0000000p-1f,0x1.0a4b300000000p-1f,
-0x1.b185d60000000p-1f,0x1.1066820000000p-1f,-0x1.ada8ee0000000p-1f,0x1.16741e0000000p-1f,-0x1.a9b6620000000p-1f,0x1.1c73b40000000p-1f,-0x1.a5ae680000000p-1f,0x1.2264f60000000p-1f,
-0x1.a191320000000p-1f,0x1.28479a0000000p-1f,-0x1.9d5ef40000000p-1f,0x1.2e1b540000000p-1f,-0x1.9917e60000000p-1f,0x1.33dfd60000000p-1f,-0x1.94bc400000000p-1f,0x1.3994d60000000p-1f,
-0x1.904c380000000p-1f,0x1.3f3a0e0000000p-1f,-0x1.8bc8060000000p-1f,0x1.44cf320000000p-1f,-0x1.872fe80000000p-1f,0x1.4a53fc0000000p-1f,-0x1.8284160000000p-1f,0x1.4fc8220000000p-1f,
-0x1.7dc4d00000000p-1f,0x1.552b600000000p-1f,-0x1.78f24e0000000p-1f,0x1.5a7d720000000p-1f,-0x1.740cd20000000p-1f,0x1.5fbe100000000p-1f,-0x1.6f149a0000000p-1f,0x1.64ecf80000000p-1f,
-0x1.6a09e60000000p-1f,0x1.6a09e60000000p-1f,-0x1.64ecf80000000p-1f,0x1.6f149a0000000p-1f,-0x1.5fbe100000000p-1f,0x1.740cd20000000p-1f,-0x1.5a7d720000000p-1f,0x1.78f24e0000000p-1f,
-0x1.552b600000000p-1f,0x1.7dc4d00000000p-1f,-0x1.4fc8220000000p-1f,0x1.8284160000000p-1f,-0x1.4a53fc0000000p-1f,0x1.872fe80000000p-1f,-0x1.44cf320000000p-1f,0x1.8bc8060000000p-1f,
-0x1.3f3a0e0000000p-1f,0x1.904c380000000p-1f,-0x1.3994d60000000p-1f,0x1.94bc400000000p-1f,-0x1.33dfd60000000p-1f,0x1.9917e60000000p-1f,-0x1.2e1b540000000p-1f,0x1.9d5ef40000000p-1f,
-0x1.28479a0000000p-1f,0x1.a191320000000p-1f,-0x1.2264f60000000p-1f,0x1.a5ae680000000p-1f,-0x1.1c73b40000000p-1f,0x1.a9b6620000000p-1f,-0x1.16741e0000000p-1f,0x1.ada8ee0000000p-1f,
-0x1.1066820000000p-1f,0x1.b185d60000000p-1f,-0x1.0a4b300000000p-1f,0x1.b54cea0000000p-1f,-0x1.0422740000000p-1f,0x1.b8fdf80000000p-1f,-0x1.fbd93e0000000p-2f,0x1.bc98d20000000p-1f,
-0x1.ef54020000000p-2f,0x1.c01d480000000p-1f,-0x1.e2b5d40000000p-2f,0x1.c38b300000000p-1f,-0x1.d5ff580000000p-2f,0x1.c6e2580000000p-1f,-0x1.c931300000000p-2f,0x1.ca229a0000000p-1f,
-0x1.bc4c040000000p-2f,0x1.cd4bca0000000p-1f,-0x1.af507a0000000p-2f,0x1.d05dc00000000p-1f,-0x1.a23f360000000p-2f,0x1.d358540000000p-1f,-0x1.9518e40000000p-2f,0x1.d63b600000000p-1f,
-0x1.87de2a0000000p-2f,0x1.d906bc0000000p-1f,-0x1.7a8fb60000000p-2f,0x1.dbba480000000p-1f,-0x1.6d2e320000000p-2f,0x1.de55e00000000p-1f,-0x1.5fba4a0000000p-2f,0x1.e0d9620000000p-1f,
-0x1.5234ac0000000p-2f,0x1.e344ae0000000p-1f,-0x1.449e080000000p-2f,0x1.e597a20000000p-1f,-0x1.36f70a0000000p-2f,0x1.e7d2240000000p-1f,-0x1.2940620000000p-2f,0x1.e9f4160000000p-1f,
-0x1.1b7ac40000000p-2f,0x1.ebfd5a0000000p-1f,-0x1.0da6e00000000p-2f,0x1.ededda0000000p-1f,-0x1.ff8ad00000000p-3f,0x1.efc57a0000000p-1f,-0x1.e3ae1c0000000p-3f,0x1.f184240000000p-1f,
-0x1.c7b90e0000000p-3f,0x1.f329c00000000p-1f,-0x1.abad0e0000000p-3f,0x1.f4b63a0000000p-1f,-0x1.8f8b840000000p-3f,0x1.f6297c0000000p-1f,-0x1.7355dc0000000p-3f,0x1.f783760000000p-1f,
-0x1.570d800000000p-3f,0x1.f8c4160000000p-1f,-0x1.3ab3e00000000p-3f,0x1.f9eb4a0000000p-1f,-0x1.1e4a660000000p-3f,0x1.faf9060000000p-1f,-0x1.01d2820000000p-3f,0x1.fbed380000000p-1f,
-0x1.ca9b440000000p-4f,0x1.fcc7d80000000p-1f,-0x1.917a6c0000000p-4f,0x1.fd88da0000000p-1f,-0x1.58455c0000000p-4f,0x1.fe30340000000p-1f,-0x1.1efef80000000p-4f,0x1.febddc0000000p-1f,
-0x1.cb54400000000p-5f,0x1.ff31cc0000000p-1f,-0x1.58936e0000000p-5f,0x1.ff8c000000000p-1f,-0x1.cb82860000000p-6f,0x1.ffcc700000000p-1f,-0x1.cb8e180000000p-7f,0x1.fff31c0000000p-1f,
0.0f,0x1.0000000000000p+0f,0x1.cb8e180000000p-7f,0x1.fff31c0000000p-1f,0x1.cb82860000000p-6f,0x1.ffcc700000000p-1f,0x1.58936e0000000p-5f,0x1.ff8c000000000p-1f,
0x1.cb54400000000p-5f,0x1.ff31cc0000000p-1f,0x1.1efef80000000p-4f,0x1.febddc0000000p-1f,0x1.58455c0000000p-4f,0x1.fe30340000000p-1f,0x1.917a6c0000000p-4f,0x1.fd88da0000000p-1f,
0x1.ca9b440000000p-4f,0x1.fcc7d80000000p-1f,0x1.01d2820000000p-3f,0x1.fbed380000000p-1f,0x1.1e4a660000000p-3f,0x1.faf9060000000p-1f,0x1.3ab3e00000000p-3f,0x1.f9eb4a0000000p-1f,
0x1.570d800000000p-3f,0x1.f8c4160000000p-1f,0x1.7355dc0000000p-3f,0x1.f783760000000p-1f,0x1.8f8b840000000p-3f,0x1.f6297c0000000p-1f,0x1.abad0e0000000p-3f,0x1.f4b63a0000000p-1f,
0x1.c7b90e0000000p-3f,0x1.f329c00000000p-1f,0x1.e3ae1c0000000p-3f,0x1.f184240000000p-1f,0x1.ff8ad00000000p-3f,0x1.efc57a0000000p-1f,0x1.0da6e00000000p-2f,0x1.ededda0000000p-1f,
0x1.1b7ac40000000p-2f,0x1.ebfd5a0000000p-1f,0x1.2940620000000p-2f,0x1.e9f4160000000p-1f,0x1.36f70a0000000p-2f,0x1.e7d2240000000p-1f,0x1.449e080000000p-2f,0x1.e597a20000000p-1f,
0x1.5234ac0000000p-2f,0x1.e344ae0000000p-1f,0x1.5fba4a0000000p-2f,0x1.e0d9620000000p-1f,0x1.6d2e320000000p-2f,0x1.de55e00000000p-1f,0x1.7a8fb60000000p-2f,0x1.dbba480000000p-1f,
0x1.87de2a0000000p-2f,0x1.d906bc0000000p-1f,0x1.9518e40000000p-2f,0x1.d63b600000000p-1f,0x1.a23f360000000p-2f,0x1.d358540000000p-1f,0x1.af507a0000000p-2f,0x1.d05dc00000000p-1f,
0x1.bc4c040000000p-2f,0x1.cd4bca0000000p-1f,0x1.c931300000000p-2f,0x1.ca229a0000000p-1f,0x1.d5ff580000000p-2f,0x1.c6e2580000000p-1f,0x1.e2b5d40000000p-2f,0x1.c38b300000000p-1f,
0x1.ef54020000000p-2f,0x1.c01d480000000p-1f,0x1.fbd93e0000000p-2f,0x1.bc98d20000000p-1f,0x1.0422740000000p-1f,0x1.b8fdf80000000p-1f,0x1.0a4b300000000p-1f,0x1.b54cea0000000p-1f,
0x1.1066820000000p-1f,0x1.b185d60000000p-1f,0x1.16741e0000000p-1f,0x1.ada8ee0000000p-1f,0x1.1c73b40000000p-1f,0x1.a9b6620000000p-1f,0x1.2264f60000000p-1f,0x1.a5ae680000000p-1f,
0x1.28479a0000000p-1f,0x1.a191320000000p-1f,0x1.2e1b540000000p-1f,0x1.9d5ef40000000p-1f,0x1.33dfd60000000p-1f,0x1.9917e60000000p-1f,0x1.3994d60000000p-1f,0x1.94bc400000000p-1f,
0x1.3f3a0e0000000p-1f,0x1.904c380000000p-1f,0x1.44cf320000000p-1f,0x1.8bc8060000000p-1f,0x1.4a53fc0000000p-1f,0x1.872fe80000000p-1f,0x1.4fc8220000000p-1f,0x1.8284160000000p-1f,
0x1.552b600000000p-1f,0x1.7dc4d00000000p-1f,0x1.5a7d720000000p-1f,0x1.78f24e0000000p-1f,0x1.5fbe100000000p-1f,0x1.740cd20000000p-1f,0x1.64ecf80000000p-1f,0x1.6f149a0000000p-1f,
0x1.6a09e60000000p-1f,0x1.6a09e60000000p-1f,0x1.6f149a0000000p-1f,0x1.64ecf80000000p-1f,0x1.740cd20000000p-1f,0x1.5fbe100000000p-1f,0x1.78f24e0000000p-1f,0x1.5a7d720000000p-1f,
0x1.7dc4d00000000p-1f,0x1.552b600000000p-1f,0x1.8284160000000p-1f,0x1.4fc8220000000p-1f,0x1.872fe80000000p-1f,0x1.4a53fc0000000p-1f,0x1.8bc8060000000p-1f,0x1.44cf320000000p-1f,
0x1.904c380000000p-1f,0x1.3f3a0e0000000p-1f,0x1.94bc400000000p-1f,0x1.3994d60000000p-1f,0x1.9917e60000000p-1f,0x1.33dfd60000000p-1f,0x1.9d5ef40000000p-1f,0x1.2e1b540000000p-1f,
0x1.a191320000000p-1f,0x1.28479a0000000p-1f,0x1.a5ae680000000p-1f,0x1.2264f60000000p-1f,0x1.a9b6620000000p-1f,0x1.1c73b40000000p-1f,0x1.ada8ee0000000p-1f,0x1.16741e0000000p-1f,
0x1.b185d60000000p-1f,0x1.1066820000000p-1f,0x1.b54cea0000000p-1f,0x1.0a4b300000000p-1f,0x1.b8fdf80000000p-1f,0x1.0422740000000p-1f,0x1.bc98d20000000p-1f,0x1.fbd93e0000000p-2f,
0x1.c01d480000000p-1f,0x1.ef54020000000p-2f,0x1.c38b300000000p-1f,0x1.e2b5d40000000p-2f,0x1.c6e2580000000p-1f,0x1.d5ff580000000p-2f,0x1.ca229a0000000p-1f,0x1.c931300000000p-2f,
0x1.cd4bca0000000p-1f,0x1.bc4c040000000p-2f,0x1.d05dc00000000p-1f,0x1.af507a0000000p-2f,0x1.d358540000000p-1f,0x1.a23f360000000p-2f,0x1.d63b600000000p-1f,0x1.9518e40000000p-2f,
0x1.d906bc0000000p-1f,0x1.87de2a0000000p-2f,0x1.dbba480000000p-1f,0x1.7a8fb60000000p-2f,0x1.de55e00000000p-1f,0x1.6d2e320000000p-2f,0x1.e0d9620000000p-1f,0x1.5fba4a0000000p-2f,
0x1.e344ae0000000p-1f,0x1.5234ac0000000p-2f,0x1.e597a20000000p-1f,0x1.449e080000000p-2f,0x1.e7d2240000000p-1f,0x1.36f70a0000000p-2f,0x1.e9f4160000000p-1f,0x1.2940620000000p-2f,
0x1.ebfd5a0000000p-1f,0x1.1b7ac40000000p-2f,0x1.ededda0000000p-1f,0x1.0da6e00000000p-2f,0x1.efc57a0000000p-1f,0x1.ff8ad00000000p-3f,0x1.f184240000000p-1f,0x1.e3ae1c0000000p-3f,
0x1.f329c00000000p-1f,0x1.c7b90e0000000p-3f,0x1.f4b63a0000000p-1f,0x1.abad0e0000000p-3f,0x1.f6297c0000000p-1f,0x1.8f8b840000000p-3f,0x1.f783760000000p-1f,0x1.7355dc0000000p-3f,
0x1.f8c4160000000p-1f,0x1.570d800000000p-3f,0x1.f9eb4a0000000p-1f,0x1.3ab3e00000000p-3f,0x1.faf9060000000p-1f,0x1.1e4a660000000p-3f,0x1.fbed380000000p-1f,0x1.01d2820000000p-3f,
0x1.fcc7d80000000p-1f,0x1.ca9b440000000p-4f,0x1.fd88da0000000p-1f,0x1.917a6c0000000p-4f,0x1.fe30340000000p-1f,0x1.58455c0000000p-4f,0x1.febddc0000000p-1f,0x1.1efef80000000p-4f,
0x1.ff31cc0000000p-1f,0x1.cb54400000000p-5f,0x1.ff8c000000000p-1f,0x1.58936e0000000p-5f,0x1.ffcc700000000p-1f,0x1.cb82860000000p-6f,0x1.fff31c0000000p-1f,0x1.cb8e180000000p-7f,
};

constexpr int S_ = 2048, D_ = 448, F_ = 1792, DF = 225;
constexpr int PF0 = 7, PF1 = 15, PF2 = 7;
constexpr size_t OFF_XNT = 0;
constexpr size_t OFF_WGU = OFF_XNT + (size_t)S_ * D_ * 2;
constexpr size_t OFF_WD = OFF_WGU + (size_t)D_ * F_ * 4;
constexpr size_t OFF_HT = OFF_WD + (size_t)D_ * F_ * 2;
constexpr size_t OFF_XT = OFF_HT + (size_t)S_ * F_ * 2;
constexpr size_t OFF_WT = OFF_XT + (size_t)DF * S_ * 8;
constexpr size_t OFF_T = OFF_WT + (size_t)DF * S_ * 4;
constexpr size_t OFF_DUM = OFF_T + (size_t)S_ * D_ * 2;
constexpr size_t OFF_FLG = OFF_DUM + 65536;

DI u32 cvtpk(float lo, float hi) { f32x2 v = {lo, hi}; bf16x2 b = __builtin_convertvector(v, bf16x2); return __builtin_bit_cast(unsigned, b); }
DI float bflo(u32 v) { return __builtin_bit_cast(float, v << 16); }
DI float bfhi(u32 v) { return __builtin_bit_cast(float, v & 0xffff0000u); }
DI float rbf(float x) { return bflo(cvtpk(x, x)); }
DI f32x2 cmul(f32x2 a, f32x2 w) { return f32x2{a.x * w.x - a.y * w.y, a.x * w.y + a.y * w.x}; }
DI f32x2 cmulc(f32x2 a, f32x2 w) { return f32x2{a.x * w.x + a.y * w.y, a.y * w.x - a.x * w.y}; }
template <bool INV> DI f32x2 mmi(f32x2 a) { return INV ? f32x2{-a.y, a.x} : f32x2{a.y, -a.x}; }
DI f32x2 tw2048(int j) { return *(const f32x2*)(TW2048 + 2 * j); }
DI f32x2 tw448(int j) { return *(const f32x2*)(TW448 + 2 * j); }
DI void block_sync() {
  __builtin_amdgcn_fence(__ATOMIC_RELEASE, "workgroup");
  __builtin_amdgcn_s_barrier();
  __builtin_amdgcn_fence(__ATOMIC_ACQUIRE, "workgroup");
}

template <bool INV> DI void dft8(f32x2* x) {
  const float r = 0.70710678118654752440f;
  f32x2 a0 = x[0] + x[4], a1 = x[0] - x[4], a2 = x[2] + x[6], a3 = mmi<INV>(x[2] - x[6]);
  f32x2 b0 = x[1] + x[5], b1 = x[1] - x[5], b2 = x[3] + x[7], b3 = mmi<INV>(x[3] - x[7]);
  f32x2 E0 = a0 + a2, E2 = a0 - a2, E1 = a1 + a3, E3 = a1 - a3;
  f32x2 O0 = b0 + b2, O2 = mmi<INV>(b0 - b2), O1 = b1 + b3, O3 = b1 - b3;
  f32x2 P1, P3;
  if (INV) { P1 = f32x2{(O1.x - O1.y) * r, (O1.x + O1.y) * r}; P3 = f32x2{(-O3.x - O3.y) * r, (O3.x - O3.y) * r}; }
  else     { P1 = f32x2{(O1.x + O1.y) * r, (O1.y - O1.x) * r}; P3 = f32x2{(O3.y - O3.x) * r, (-O3.x - O3.y) * r}; }
  x[0] = E0 + O0; x[4] = E0 - O0; x[1] = E1 + P1; x[5] = E1 - P1;
  x[2] = E2 + O2; x[6] = E2 - O2; x[3] = E3 + P3; x[7] = E3 - P3;
}
template <bool INV> DI void dft4(f32x2* x) {
  f32x2 a0 = x[0] + x[2], a1 = x[0] - x[2], a2 = x[1] + x[3], a3 = mmi<INV>(x[1] - x[3]);
  x[0] = a0 + a2; x[2] = a0 - a2; x[1] = a1 + a3; x[3] = a1 - a3;
}
template <bool INV> DI void dft7(f32x2* x) {
  const float c1 = 0.62348980185873353053f, c2 = -0.22252093395631440429f, c3 = -0.90096886790241912624f;
  const float s1 = 0.78183148246802980871f, s2 = 0.97492791218182360702f, s3 = 0.43388373911755812048f;
  f32x2 t1 = x[1] + x[6], t2 = x[2] + x[5], t3 = x[3] + x[4];
  f32x2 u1 = x[1] - x[6], u2 = x[2] - x[5], u3 = x[3] - x[4];
  f32x2 x0 = x[0];
  f32x2 R1 = x0 + c1 * t1 + c2 * t2 + c3 * t3;
  f32x2 R2 = x0 + c2 * t1 + c3 * t2 + c1 * t3;
  f32x2 R3 = x0 + c3 * t1 + c1 * t2 + c2 * t3;
  f32x2 I1 = mmi<INV>(s1 * u1 + s2 * u2 + s3 * u3);
  f32x2 I2 = mmi<INV>(s2 * u1 - s3 * u2 - s1 * u3);
  f32x2 I3 = mmi<INV>(s3 * u1 - s1 * u2 + s2 * u3);
  x[0] = x0 + t1 + t2 + t3;
  x[1] = R1 + I1; x[6] = R1 - I1; x[2] = R2 + I2; x[5] = R2 - I2; x[3] = R3 + I3; x[4] = R3 - I3;
}
template <int CTRL> DI float dpp_add(float v) {
  return v + __builtin_bit_cast(float, __builtin_amdgcn_update_dpp(0, __builtin_bit_cast(int, v), CTRL, 0xf, 0xf, true));
}
DI float wave_sum(float v, int lane) {
  v = dpp_add<0x111>(v);
  v = dpp_add<0x112>(v);
  v = dpp_add<0x114>(v);
  v = dpp_add<0x118>(v);
  v = dpp_add<0x142>(v);
  v = dpp_add<0x143>(v);
  return __builtin_bit_cast(float, __builtin_amdgcn_readlane(__builtin_bit_cast(int, v), 63));
}
DI void wave_lds_fence() { asm volatile("" ::: "memory"); __builtin_amdgcn_wave_barrier(); }

DI void glds16(const void* g, void* l) {
  __builtin_amdgcn_global_load_lds((const __attribute__((address_space(1))) void*)g, (__attribute__((address_space(3))) void*)l, 16, 0, 0);
}
template <int NKB> DI void code_prefetch(void* lds_dummy, int w, int lane) {
  const char* pc = (const char*)(__builtin_amdgcn_s_getpc() & ~(uint64_t)1023);
#pragma unroll
  for (int j = 0; j < (NKB + 3) / 4; ++j) {
    const int kb = 4 * j + w;
    if (kb < NKB) glds16(pc + kb * 1024 + lane * 16, lds_dummy);
  }
}
constexpr int K0_LN = 256, K0_W = 98, K0_T = 256;

DI void transpose8x8(const u32x4* r, u32x4* c) {
#pragma unroll
  for (int j = 0; j < 8; ++j) {
#pragma unroll
    for (int m = 0; m < 4; ++m)
      c[j][m] = __builtin_amdgcn_perm(r[2 * m + 1][j >> 1], r[2 * m][j >> 1], (j & 1) ? 0x07060302u : 0x05040100u);
  }
}

__global__ __launch_bounds__(256) void k0_prep(const u16* __restrict__ x, const u16* __restrict__ wr, const u16* __restrict__ wi,
                                               const u16* __restrict__ wg, const u16* __restrict__ wu,
                                               char* __restrict__ ws) {
  __shared__ __attribute__((aligned(16))) char smem[4 * 4096 + 8 * 56 * 16 + DF * 8 * 8 + 1024];
  const int tid = __builtin_amdgcn_workitem_id_x(), w = __builtin_amdgcn_readfirstlane(tid >> 6), lane = tid & 63;
  const int b = __builtin_amdgcn_workgroup_id_x();
  STAMP(0, 0);
  code_prefetch<PF0>(smem + 4 * 4096 + 8 * 56 * 16 + DF * 8 * 8, w, lane);
  if (b < K0_LN) {
    f32x2* zb = (f32x2*)(smem + w * 4096);
    u32x4* xs = (u32x4*)(smem + 4 * 4096);
    f32x2* tt = (f32x2*)(smem + 4 * 4096 + 8 * 56 * 16);
    const int r0 = 8 * b, ra = r0 + 2 * w;
    const bool act = lane < 56;
    const int k1 = act ? (lane >> 3) : 0, a = lane & 7;
    u32x4 va = {0, 0, 0, 0}, vb = {0, 0, 0, 0};
    if (act) { va = *(const u32x4*)(x + (size_t)ra * D_ + lane * 8); vb = *(const u32x4*)(x + (size_t)(ra + 1) * D_ + lane * 8); }
    f32x2 tA[8], tB[8];
#pragma unroll
    for (int kk = 1; kk < 7; ++kk) tA[kk] = tw448(lane * kk);
#pragma unroll
    for (int c = 1; c < 8; ++c) tB[c] = tw448(7 * a * c);
    float fa[8], fb[8];
#pragma unroll
    for (int j = 0; j < 4; ++j) { fa[2 * j] = bflo(va[j]); fa[2 * j + 1] = bfhi(va[j]); fb[2 * j] = bflo(vb[j]); fb[2 * j + 1] = bfhi(vb[j]); }
    float sa = 0.f, sb = 0.f;
#pragma unroll
    for (int j = 0; j < 8; ++j) { sa += fa[j]; sb += fb[j]; }
    sa = wave_sum(sa, lane); sb = wave_sum(sb, lane);
    const float ma = sa * (1.f / 448.f), mb = sb * (1.f / 448.f);
    const float mab = rbf(ma), mbb = rbf(mb);
    float qa = 0.f, qb = 0.f;
#pragma unroll
    for (int j = 0; j < 8; ++j) {
      float da = act ? fa[j] - ma : 0.f, db = act ? fb[j] - mb : 0.f;
      qa += da * da; qb += db * db;
    }
    qa = wave_sum(qa, lane); qb = wave_sum(qb, lane);
    const float sda = rbf(__builtin_sqrtf(rbf(rbf(qa * (1.f / 448.f)) + 1e-5f)));
    const float sdb = rbf(__builtin_sqrtf(rbf(rbf(qb * (1.f / 448.f)) + 1e-5f)));
    u32x4 na, nb;
#pragma unroll
    for (int j = 0; j < 4; ++j) {
      u32 ea = cvtpk(fa[2 * j] - mab, fa[2 * j + 1] - mab), eb = cvtpk(fb[2 * j] - mbb, fb[2 * j + 1] - mbb);
      na[j] = cvtpk(bflo(ea) / sda, bfhi(ea) / sda);
      nb[j] = cvtpk(bflo(eb) / sdb, bfhi(eb) / sdb);
    }
    if (act) {
      xs[(2 * w) * 56 + lane] = na; xs[(2 * w + 1) * 56 + lane] = nb;
#pragma unroll
      for (int j = 0; j < 4; ++j) {
        f32x4 z2 = {bflo(na[j]), bflo(nb[j]), bfhi(na[j]), bfhi(nb[j])};
        *(f32x4*)(zb + 8 * lane + 2 * j) = z2;
      }
    }
    wave_lds_fence();
    STAMP(0, 2);
    f32x2 v[8];
#pragma unroll
    for (int n1 = 0; n1 < 7; ++n1) v[n1] = zb[64 * n1 + lane];
    dft7<false>(v);
#pragma unroll
    for (int kk = 1; kk < 7; ++kk) v[kk] = cmul(v[kk], tA[kk]);
    wave_lds_fence();
#pragma unroll
    for (int kk = 0; kk < 7; ++kk) zb[72 * kk + lane] = v[kk];
    wave_lds_fence();
#pragma unroll
    for (int bb = 0; bb < 8; ++bb) v[bb] = zb[72 * k1 + a + 8 * bb];
    dft8<false>(v);
#pragma unroll
    for (int c = 1; c < 8; ++c) v[c] = cmul(v[c], tB[c]);
    wave_lds_fence();
    if (act) {
#pragma unroll
      for (int c = 0; c < 8; ++c) zb[72 * k1 + 9 * a + c] = v[c];
    }
    wave_lds_fence();
#pragma unroll
    for (int aa = 0; aa < 8; ++aa) v[aa] = zb[72 * k1 + 9 * aa + a];
    dft8<false>(v);
    wave_lds_fence();
    if (act) {
#pragma unroll
      for (int d = 0; d < 8; ++d) zb[k1 + 7 * a + 56 * d] = v[d];
    }
    wave_lds_fence();
#pragma unroll
    for (int i = 0; i < 4; ++i) {
      const int k = lane + 64 * i;
      if (k < DF) {
        f32x2 zk = zb[k], zn = zb[k == 0 ? 0 : 448 - k];
        f32x4 o = {0.5f * (zk.x + zn.x), 0.5f * (zk.y - zn.y), 0.5f * (zk.y + zn.y), 0.5f * (zn.x - zk.x)};
        *(f32x4*)(tt + k * 8 + 2 * w) = o;
      }
    }
    block_sync();
    STAMP(0, 3);
    f32x2* XT = (f32x2*)(ws + OFF_XT);
#pragma unroll
    for (int i = 0; i < 4; ++i) {
      const int e = tid + 256 * i;
      if (e < DF * 4) { const int k = e >> 2, p = e & 3; *(f32x4*)(XT + (size_t)k * S_ + r0 + 2 * p) = *(const f32x4*)(tt + k * 8 + 2 * p); }
    }
    u32x4* XNT = (u32x4*)(ws + OFF_XNT);
#pragma unroll
    for (int i = 0; i < 2; ++i) {
      const int e = tid + 256 * i;
      if (e < 448) {
        const int r = e & 7, c = e >> 3, row = r0 + r;
        XNT[((row >> 4) * 14 + (c >> 2)) * 64 + (c & 3) * 16 + (row & 15)] = xs[r * 56 + c];
      }
    }
  } else if (b < K0_LN + K0_W) {
    const int g = (b - K0_LN) * 256 + tid;
    u32x4 r[8], c[8];
    {
      const int gu = g >= 12544, e = g - gu * 12544, kb = e / 224, fb = e - kb * 224;
      const u16* src = (gu ? wu : wg) + (size_t)(8 * kb) * F_ + 8 * fb;
#pragma unroll
      for (int i = 0; i < 8; ++i) r[i] = *(const u32x4*)(src + (size_t)i * F_);
      transpose8x8(r, c);
      const int s = kb >> 2, q = kb & 3, f0 = 8 * fb, ft = f0 >> 5, tl = (f0 >> 4) & 1;
      u32x4* dst = (u32x4*)(ws + OFF_WGU) + (((ft * 14 + s) * 2 + tl) * 2 + gu) * 64 + q * 16 + (f0 & 15);
#pragma unroll
      for (int j = 0; j < 8; ++j) dst[j] = c[j];
    }
  } else {
    const int bb = b - K0_LN - K0_W, pos0 = 8 * bb;
    if (bb == 0) ((u32*)(ws + OFF_FLG))[tid * 16] = 0u;
    if (tid < DF) {
      u32 o[8];
#pragma unroll
      for (int p = 0; p < 8; ++p) {
        const int pos = pos0 + p, T = pos >> 3, vv = pos & 7;
        const int k1 = (T >> 5) + 8 * ((T & 31) >> 2) + 64 * (2 * (T & 3) + (vv >> 2)) + 512 * (vv & 3);
        o[p] = (u32)wr[(size_t)k1 * DF + tid] | ((u32)wi[(size_t)k1 * DF + tid] << 16);
      }
      u32x4* dst = (u32x4*)(ws + OFF_WT + ((size_t)tid * S_ + pos0) * 4);
#pragma unroll
      for (int i = 0; i < 2; ++i) dst[i] = u32x4{o[4 * i], o[4 * i + 1], o[4 * i + 2], o[4 * i + 3]};
    }
  }
  STAMP(0, 1);
}

constexpr int K1_G = 224, K1_P = 225;
#define MFMA32(a, b, c) __builtin_amdgcn_mfma_f32_32x32x16_bf16((a), (b), (c), 0, 0, 0)
DI float gelu_mul(float g, float u) {
  const float k1 = -2.f * 0.7978845608028654f * 1.4426950408889634f, k3 = k1 * 0.044715f;
  float z = g * (k1 + k3 * g * g);
  float e = __builtin_amdgcn_exp2f(z);
  float gl = g * __builtin_amdgcn_rcpf(1.f + e);
  return rbf(gl) * u;
}

#define MFMA16(a, b, c) __builtin_amdgcn_mfma_f32_16x16x32_bf16((a), (b), (c), 0, 0, 0)
__global__ NOPK __launch_bounds__(512, 4) void k1_gemm1_colfft(const u16* __restrict__ wd, char* __restrict__ ws) {
  __shared__ __attribute__((aligned(16))) char smem[81920];
  const int tid = __builtin_amdgcn_workitem_id_x(), w = __builtin_amdgcn_readfirstlane(tid >> 6), lane = tid & 63;
  const int b = __builtin_amdgcn_workgroup_id_x();
  const unsigned vo16 = lane * 16;
  const unsigned lds0 = (unsigned)(size_t)(__attribute__((address_space(3))) char*)smem;
  STAMP(1, 0);
  code_prefetch<PF1>(smem + 4 * 16384 + w * 1024, w >> 1, lane);
  if (b < K1_G) {
    const int xcd = b & 7, i = b >> 3;
    const int rt128 = 4 * (xcd & 3) + (i & 3), ft128 = 7 * (xcd >> 2) + (i >> 2);
    const int rt16 = 8 * rt128 + w;
    const char* wbase = ws + OFF_WGU + (size_t)ft128 * (4 * 14 * 4096) + w * 2048;
    const unsigned ldw = lds0 + w * 2048;
#define G1_LOAD(st, slot) do { const char* p_ = wbase + (st) * 16384; const unsigned l_ = ldw + (slot) * 16384; \
      asm volatile("s_mov_b32 m0, %0\n\ts_nop 0\n\tglobal_load_lds_dwordx4 %1, %2\n\tglobal_load_lds_dwordx4 %1, %2 offset:1024" :: "s"(l_), "v"(vo16), "s"(p_) : "memory"); } while (0)
    bf16x8 xf[14];
    const auto rs = __builtin_amdgcn_make_buffer_rsrc(ws, 0, 0x10000000, 0x00020000);
    const unsigned xso = (unsigned)OFF_XNT + rt16 * 14 * 1024;
#pragma unroll
    for (int s = 0; s < 14; ++s) xf[s] = __builtin_bit_cast(bf16x8, __builtin_amdgcn_raw_buffer_load_b128(rs, vo16, xso + s * 1024, 0));
    asm volatile("" ::: "memory");
    G1_LOAD(0, 0); G1_LOAD(1, 1); G1_LOAD(2, 2); G1_LOAD(3, 3);
    const unsigned hso = (unsigned)OFF_HT + ((rt16 >> 1) * 112 + 8 * ft128) * 1024 + (rt16 & 1) * 256;
    f32x4 ac[4];
    const f32x4 zero = {0.f, 0.f, 0.f, 0.f};
    u32x4 gpk = {0, 0, 0, 0}, upk = {0, 0, 0, 0};
    bf16x8 fr[2][4];
    float ez0, ez1;
    const unsigned la0 = lds0 + vo16, la4 = la0 + 65536;
    asm volatile("s_waitcnt vmcnt(6)" ::: "memory");
    __builtin_amdgcn_s_barrier();
    asm volatile("" ::: "memory");
    STAMP(1, 2);
#define DSR(dst, addr, off) asm volatile("ds_read_b128 %0, %1 offset:" #off : "=v"(dst) : "v"(addr))
    DSR(fr[0][0], la0, 0); DSR(fr[0][1], la0, 1024); DSR(fr[0][2], la0, 2048); DSR(fr[0][3], la0, 3072);
#define EPI_PACK() do { _Pragma("unroll") for (int p_ = 0; p_ < 4; ++p_) { \
      gpk[p_] = cvtpk(ac[2 * (p_ >> 1)][2 * (p_ & 1)], ac[2 * (p_ >> 1)][2 * (p_ & 1) + 1]); \
      upk[p_] = cvtpk(ac[2 * (p_ >> 1) + 1][2 * (p_ & 1)], ac[2 * (p_ >> 1) + 1][2 * (p_ & 1) + 1]); } } while (0)
#define EPI_A(p) do { \
      const float k1_ = -2.f * 0.7978845608028654f * 1.4426950408889634f, k3_ = k1_ * 0.044715f; \
      const float eg0 = bflo(gpk[p]), eg1 = bfhi(gpk[p]); \
      ez0 = eg0 * (k1_ + k3_ * eg0 * eg0); ez1 = eg1 * (k1_ + k3_ * eg1 * eg1); } while (0)
#define EPI_B(p) do { \
      ez0 = bflo(gpk[p]) * __builtin_amdgcn_rcpf(1.f + __builtin_amdgcn_exp2f(ez0)); ez1 = bfhi(gpk[p]) * __builtin_amdgcn_rcpf(1.f + __builtin_amdgcn_exp2f(ez1)); } while (0)
#define EPI_C(p, T) do { \
      u32 glp_ = cvtpk(ez0, ez1); \
      gpk[p] = cvtpk(bflo(glp_) * bflo(upk[p]), bfhi(glp_) * bfhi(upk[p])); \
      if ((p) == 3) { unsigned l16_ = vo16; asm volatile("" : "+v"(l16_));   \
        const unsigned hvo_ = ((l16_ & 512u) << 1) + ((l16_ & 256u) << 1) + (l16_ & 255u); \
        __builtin_amdgcn_raw_buffer_store_b128(gpk, rs, hvo_, hso + ((T) & 3) * 2048, 0); } } while (0)
#define EPI_STEP(s, T) do { if ((s) >= 1 && (s) <= 12) { \
      if (((s) - 1) % 3 == 0) EPI_A(((s) - 1) / 3); else if (((s) - 1) % 3 == 1) EPI_B(((s) - 1) / 3); else EPI_C(((s) - 1) / 3, T); } } while (0)
      asm volatile("s_waitcnt vmcnt(4)" ::: "memory");
      __builtin_amdgcn_s_barrier();
      G1_LOAD(4, 4);
      asm volatile("s_waitcnt lgkmcnt(0)" ::: "memory");
      DSR(fr[1][0], la0, 4096);
      DSR(fr[1][1], la0, 5120);
      DSR(fr[1][2], la0, 6144);
      DSR(fr[1][3], la0, 7168);
      __builtin_amdgcn_sched_barrier(0);
      ac[0] = MFMA16(fr[0][0], xf[0], zero);
      ac[1] = MFMA16(fr[0][1], xf[0], zero);
      ac[2] = MFMA16(fr[0][2], xf[0], zero);
      ac[3] = MFMA16(fr[0][3], xf[0], zero);
      __builtin_amdgcn_sched_barrier(0);
      asm volatile("s_waitcnt lgkmcnt(0)" ::: "memory");
      DSR(fr[0][0], la0, 8192);
      DSR(fr[0][1], la0, 9216);
      DSR(fr[0][2], la0, 10240);
      DSR(fr[0][3], la0, 11264);
      __builtin_amdgcn_sched_barrier(0);
      ac[0] = MFMA16(fr[1][0], xf[1], ac[0]);
      ac[1] = MFMA16(fr[1][1], xf[1], ac[1]);
      ac[2] = MFMA16(fr[1][2], xf[1], ac[2]);
      ac[3] = MFMA16(fr[1][3], xf[1], ac[3]);
      __builtin_amdgcn_sched_barrier(0);
      asm volatile("s_waitcnt lgkmcnt(0)" ::: "memory");
      DSR(fr[1][0], la0, 12288);
      DSR(fr[1][1], la0, 13312);
      DSR(fr[1][2], la0, 14336);
      DSR(fr[1][3], la0, 15360);
      __builtin_amdgcn_sched_barrier(0);
      ac[0] = MFMA16(fr[0][0], xf[2], ac[0]);
      ac[1] = MFMA16(fr[0][1], xf[2], ac[1]);
      ac[2] = MFMA16(fr[0][2], xf[2], ac[2]);
      ac[3] = MFMA16(fr[0][3], xf[2], ac[3]);
      __builtin_amdgcn_sched_barrier(0);
      asm volatile("s_waitcnt lgkmcnt(0)" ::: "memory");
      DSR(fr[0][0], la0, 16384);
      DSR(fr[0][1], la0, 17408);
      DSR(fr[0][2], la0, 18432);
      DSR(fr[0][3], la0, 19456);
      __builtin_amdgcn_sched_barrier(0);
      ac[0] = MFMA16(fr[1][0], xf[3], ac[0]);
      ac[1] = MFMA16(fr[1][1], xf[3], ac[1]);
      ac[2] = MFMA16(fr[1][2], xf[3], ac[2]);
      ac[3] = MFMA16(fr[1][3], xf[3], ac[3]);
      __builtin_amdgcn_sched_barrier(0);
      asm volatile("s_waitcnt vmcnt(4)" ::: "memory");
      __builtin_amdgcn_s_barrier();
      G1_LOAD(5, 0);
      asm volatile("s_waitcnt lgkmcnt(0)" ::: "memory");
      DSR(fr[1][0], la0, 20480);
      DSR(fr[1][1], la0, 21504);
      DSR(fr[1][2], la0, 22528);
      DSR(fr[1][3], la0, 23552);
      __builtin_amdgcn_sched_barrier(0);
      ac[0] = MFMA16(fr[0][0], xf[4], ac[0]);
      ac[1] = MFMA16(fr[0][1], xf[4], ac[1]);
      ac[2] = MFMA16(fr[0][2], xf[4], ac[2]);
      ac[3] = MFMA16(fr[0][3], xf[4], ac[3]);
      __builtin_amdgcn_sched_barrier(0);
      asm volatile("s_waitcnt lgkmcnt(0)" ::: "memory");
      DSR(fr[0][0], la0, 24576);
      DSR(fr[0][1], la0, 25600);
      DSR(fr[0][2], la0, 26624);
      DSR(fr[0][3], la0, 27648);
      __builtin_amdgcn_sched_barrier(0);
      ac[0] = MFMA16(fr[1][0], xf[5], ac[0]);
      ac[1] = MFMA16(fr[1][1], xf[5], ac[1]);
      ac[2] = MFMA16(fr[1][2], xf[5], ac[2]);
      ac[3] = MFMA16(fr[1][3], xf[5], ac[3]);
      __builtin_amdgcn_sched_barrier(0);
      asm volatile("s_waitcnt lgkmcnt(0)" ::: "memory");
      DSR(fr[1][0], la0, 28672);
      DSR(fr[1][1], la0, 29696);
      DSR(fr[1][2], la0, 30720);
      DSR(fr[1][3], la0, 31744);
      __builtin_amdgcn_sched_barrier(0);
      ac[0] = MFMA16(fr[0][0], xf[6], ac[0]);
      ac[1] = MFMA16(fr[0][1], xf[6], ac[1]);
      ac[2] = MFMA16(fr[0][2], xf[6], ac[2]);
      ac[3] = MFMA16(fr[0][3], xf[6], ac[3]);
      __builtin_amdgcn_sched_barrier(0);
      asm volatile("s_waitcnt lgkmcnt(0)" ::: "memory");
      DSR(fr[0][0], la0, 32768);
      DSR(fr[0][1], la0, 33792);
      DSR(fr[0][2], la0, 34816);
      DSR(fr[0][3], la0, 35840);
      __builtin_amdgcn_sched_barrier(0);
      ac[0] = MFMA16(fr[1][0], xf[7], ac[0]);
      ac[1] = MFMA16(fr[1][1], xf[7], ac[1]);
      ac[2] = MFMA16(fr[1][2], xf[7], ac[2]);
      ac[3] = MFMA16(fr[1][3], xf[7], ac[3]);
      __builtin_amdgcn_sched_barrier(0);
      asm volatile("s_waitcnt vmcnt(4)" ::: "memory");
      __builtin_amdgcn_s_barrier();
      G1_LOAD(6, 1);
      asm volatile("s_waitcnt lgkmcnt(0)" ::: "memory");
      DSR(fr[1][0], la0, 36864);
      DSR(fr[1][1], la0, 37888);
      DSR(fr[1][2], la0, 38912);
      DSR(fr[1][3], la0, 39936);
      __builtin_amdgcn_sched_barrier(0);
      ac[0] = MFMA16(fr[0][0], xf[8], ac[0]);
      ac[1] = MFMA16(fr[0][1], xf[8], ac[1]);
      ac[2] = MFMA16(fr[0][2], xf[8], ac[2]);
      ac[3] = MFMA16(fr[0][3], xf[8], ac[3]);
      __builtin_amdgcn_sched_barrier(0);
      asm volatile("s_waitcnt lgkmcnt(0)" ::: "memory");
      DSR(fr[0][0], la0, 40960);
      DSR(fr[0][1], la0, 41984);
      DSR(fr[0][2], la0, 43008);
      DSR(fr[0][3], la0, 44032);
      __builtin_amdgcn_sched_barrier(0);
      ac[0] = MFMA16(fr[1][0], xf[9], ac[0]);
      ac[1] = MFMA16(fr[1][1], xf[9], ac[1]);
      ac[2] = MFMA16(fr[1][2], xf[9], ac[2]);
      ac[3] = MFMA16(fr[1][3], xf[9], ac[3]);
      __builtin_amdgcn_sched_barrier(0);
      asm volatile("s_waitcnt lgkmcnt(0)" ::: "memory");
      DSR(fr[1][0], la0, 45056);
      DSR(fr[1][1], la0, 46080);
      DSR(fr[1][2], la0, 47104);
      DSR(fr[1][3], la0, 48128);
      __builtin_amdgcn_sched_barrier(0);
      ac[0] = MFMA16(fr[0][0], xf[10], ac[0]);
      ac[1] = MFMA16(fr[0][1], xf[10], ac[1]);
      ac[2] = MFMA16(fr[0][2], xf[10], ac[2]);
      ac[3] = MFMA16(fr[0][3], xf[10], ac[3]);
      __builtin_amdgcn_sched_barrier(0);
      asm volatile("s_waitcnt lgkmcnt(0)" ::: "memory");
      DSR(fr[0][0], la0, 49152);
      DSR(fr[0][1], la0, 50176);
      DSR(fr[0][2], la0, 51200);
      DSR(fr[0][3], la0, 52224);
      __builtin_amdgcn_sched_barrier(0);
      ac[0] = MFMA16(fr[1][0], xf[11], ac[0]);
      ac[1] = MFMA16(fr[1][1], xf[11], ac[1]);
      ac[2] = MFMA16(fr[1][2], xf[11], ac[2]);
      ac[3] = MFMA16(fr[1][3], xf[11], ac[3]);
      __builtin_amdgcn_sched_barrier(0);
      asm volatile("s_waitcnt vmcnt(4)" ::: "memory");
      __builtin_amdgcn_s_barrier();
      G1_LOAD(7, 2);
      asm volatile("s_waitcnt lgkmcnt(0)" ::: "memory");
      DSR(fr[1][0], la0, 53248);
      DSR(fr[1][1], la0, 54272);
      DSR(fr[1][2], la0, 55296);
      DSR(fr[1][3], la0, 56320);
      __builtin_amdgcn_sched_barrier(0);
      ac[0] = MFMA16(fr[0][0], xf[12], ac[0]);
      ac[1] = MFMA16(fr[0][1], xf[12], ac[1]);
      ac[2] = MFMA16(fr[0][2], xf[12], ac[2]);
      ac[3] = MFMA16(fr[0][3], xf[12], ac[3]);
      __builtin_amdgcn_sched_barrier(0);
      asm volatile("s_waitcnt lgkmcnt(0)" ::: "memory");
      DSR(fr[0][0], la0, 57344);
      DSR(fr[0][1], la0, 58368);
      DSR(fr[0][2], la0, 59392);
      DSR(fr[0][3], la0, 60416);
      __builtin_amdgcn_sched_barrier(0);
      ac[0] = MFMA16(fr[1][0], xf[13], ac[0]);
      ac[1] = MFMA16(fr[1][1], xf[13], ac[1]);
      ac[2] = MFMA16(fr[1][2], xf[13], ac[2]);
      ac[3] = MFMA16(fr[1][3], xf[13], ac[3]);
      __builtin_amdgcn_sched_barrier(0);
      EPI_PACK();
      asm volatile("s_waitcnt lgkmcnt(0)" ::: "memory");
      DSR(fr[1][0], la0, 61440);
      DSR(fr[1][1], la0, 62464);
      DSR(fr[1][2], la0, 63488);
      DSR(fr[1][3], la0, 64512);
      __builtin_amdgcn_sched_barrier(0);
      ac[0] = MFMA16(fr[0][0], xf[0], zero);
      ac[1] = MFMA16(fr[0][1], xf[0], zero);
      ac[2] = MFMA16(fr[0][2], xf[0], zero);
      ac[3] = MFMA16(fr[0][3], xf[0], zero);
      EPI_STEP(0, 0);
      __builtin_amdgcn_sched_barrier(0);
      asm volatile("s_waitcnt lgkmcnt(0)" ::: "memory");
      DSR(fr[0][0], la4, 0);
      DSR(fr[0][1], la4, 1024);
      DSR(fr[0][2], la4, 2048);
      DSR(fr[0][3], la4, 3072);
      __builtin_amdgcn_sched_barrier(0);
      ac[0] = MFMA16(fr[1][0], xf[1], ac[0]);
      ac[1] = MFMA16(fr[1][1], xf[1], ac[1]);
      ac[2] = MFMA16(fr[1][2], xf[1], ac[2]);
      ac[3] = MFMA16(fr[1][3], xf[1], ac[3]);
      EPI_STEP(1, 0);
      __builtin_amdgcn_sched_barrier(0);
      asm volatile("s_waitcnt vmcnt(4)" ::: "memory");
      __builtin_amdgcn_s_barrier();
      G1_LOAD(8, 3);
      asm volatile("s_waitcnt lgkmcnt(0)" ::: "memory");
      DSR(fr[1][0], la4, 4096);
      DSR(fr[1][1], la4, 5120);
      DSR(fr[1][2], la4, 6144);
      DSR(fr[1][3], la4, 7168);
      __builtin_amdgcn_sched_barrier(0);
      ac[0] = MFMA16(fr[0][0], xf[2], ac[0]);
      ac[1] = MFMA16(fr[0][1], xf[2], ac[1]);
      ac[2] = MFMA16(fr[0][2], xf[2], ac[2]);
      ac[3] = MFMA16(fr[0][3], xf[2], ac[3]);
      EPI_STEP(2, 0);
      __builtin_amdgcn_sched_barrier(0);
      asm volatile("s_waitcnt lgkmcnt(0)" ::: "memory");
      DSR(fr[0][0], la4, 8192);
      DSR(fr[0][1], la4, 9216);
      DSR(fr[0][2], la4, 10240);
      DSR(fr[0][3], la4, 11264);
      __builtin_amdgcn_sched_barrier(0);
      ac[0] = MFMA16(fr[1][0], xf[3], ac[0]);
      ac[1] = MFMA16(fr[1][1], xf[3], ac[1]);
      ac[2] = MFMA16(fr[1][2], xf[3], ac[2]);
      ac[3] = MFMA16(fr[1][3], xf[3], ac[3]);
      EPI_STEP(3, 0);
      __builtin_amdgcn_sched_barrier(0);
      asm volatile("s_waitcnt lgkmcnt(0)" ::: "memory");
      DSR(fr[1][0], la4, 12288);
      DSR(fr[1][1], la4, 13312);
      DSR(fr[1][2], la4, 14336);
      DSR(fr[1][3], la4, 15360);
      __builtin_amdgcn_sched_barrier(0);
      ac[0] = MFMA16(fr[0][0], xf[4], ac[0]);
      ac[1] = MFMA16(fr[0][1], xf[4], ac[1]);
      ac[2] = MFMA16(fr[0][2], xf[4], ac[2]);
      ac[3] = MFMA16(fr[0][3], xf[4], ac[3]);
      EPI_STEP(4, 0);
      __builtin_amdgcn_sched_barrier(0);
      asm volatile("s_waitcnt lgkmcnt(0)" ::: "memory");
      DSR(fr[0][0], la0, 0);
      DSR(fr[0][1], la0, 1024);
      DSR(fr[0][2], la0, 2048);
      DSR(fr[0][3], la0, 3072);
      __builtin_amdgcn_sched_barrier(0);
      ac[0] = MFMA16(fr[1][0], xf[5], ac[0]);
      ac[1] = MFMA16(fr[1][1], xf[5], ac[1]);
      ac[2] = MFMA16(fr[1][2], xf[5], ac[2]);
      ac[3] = MFMA16(fr[1][3], xf[5], ac[3]);
      EPI_STEP(5, 0);
      __builtin_amdgcn_sched_barrier(0);
      asm volatile("s_waitcnt vmcnt(4)" ::: "memory");
      __builtin_amdgcn_s_barrier();
      G1_LOAD(9, 4);
      asm volatile("s_waitcnt lgkmcnt(0)" ::: "memory");
      DSR(fr[1][0], la0, 4096);
      DSR(fr[1][1], la0, 5120);
      DSR(fr[1][2], la0, 6144);
      DSR(fr[1][3], la0, 7168);
      __builtin_amdgcn_sched_barrier(0);
      ac[0] = MFMA16(fr[0][0], xf[6], ac[0]);
      ac[1] = MFMA16(fr[0][1], xf[6], ac[1]);
      ac[2] = MFMA16(fr[0][2], xf[6], ac[2]);
      ac[3] = MFMA16(fr[0][3], xf[6], ac[3]);
      EPI_STEP(6, 0);
      __builtin_amdgcn_sched_barrier(0);
      asm volatile("s_waitcnt lgkmcnt(0)" ::: "memory");
      DSR(fr[0][0], la0, 8192);
      DSR(fr[0][1], la0, 9216);
      DSR(fr[0][2], la0, 10240);
      DSR(fr[0][3], la0, 11264);
      __builtin_amdgcn_sched_barrier(0);
      ac[0] = MFMA16(fr[1][0], xf[7], ac[0]);
      ac[1] = MFMA16(fr[1][1], xf[7], ac[1]);
      ac[2] = MFMA16(fr[1][2], xf[7], ac[2]);
      ac[3] = MFMA16(fr[1][3], xf[7], ac[3]);
      EPI_STEP(7, 0);
      __builtin_amdgcn_sched_barrier(0);
      asm volatile("s_waitcnt lgkmcnt(0)" ::: "memory");
      DSR(fr[1][0], la0, 12288);
      DSR(fr[1][1], la0, 13312);
      DSR(fr[1][2], la0, 14336);
      DSR(fr[1][3], la0, 15360);
      __builtin_amdgcn_sched_barrier(0);
      ac[0] = MFMA16(fr[0][0], xf[8], ac[0]);
      ac[1] = MFMA16(fr[0][1], xf[8], ac[1]);
      ac[2] = MFMA16(fr[0][2], xf[8], ac[2]);
      ac[3] = MFMA16(fr[0][3], xf[8], ac[3]);
      EPI_STEP(8, 0);
      __builtin_amdgcn_sched_barrier(0);
      asm volatile("s_waitcnt lgkmcnt(0)" ::: "memory");
      DSR(fr[0][0], la0, 16384);
      DSR(fr[0][1], la0, 17408);
      DSR(fr[0][2], la0, 18432);
      DSR(fr[0][3], la0, 19456);
      __builtin_amdgcn_sched_barrier(0);
      ac[0] = MFMA16(fr[1][0], xf[9], ac[0]);
      ac[1] = MFMA16(fr[1][1], xf[9], ac[1]);
      ac[2] = MFMA16(fr[1][2], xf[9], ac[2]);
      ac[3] = MFMA16(fr[1][3], xf[9], ac[3]);
      EPI_STEP(9, 0);
      __builtin_amdgcn_sched_barrier(0);
      asm volatile("s_waitcnt vmcnt(4)" ::: "memory");
      __builtin_amdgcn_s_barrier();
      G1_LOAD(10, 0);
      asm volatile("s_waitcnt lgkmcnt(0)" ::: "memory");
      DSR(fr[1][0], la0, 20480);
      DSR(fr[1][1], la0, 21504);
      DSR(fr[1][2], la0, 22528);
      DSR(fr[1][3], la0, 23552);
      __builtin_amdgcn_sched_barrier(0);
      ac[0] = MFMA16(fr[0][0], xf[10], ac[0]);
      ac[1] = MFMA16(fr[0][1], xf[10], ac[1]);
      ac[2] = MFMA16(fr[0][2], xf[10], ac[2]);
      ac[3] = MFMA16(fr[0][3], xf[10], ac[3]);
      EPI_STEP(10, 0);
      __builtin_amdgcn_sched_barrier(0);
      asm volatile("s_waitcnt lgkmcnt(0)" ::: "memory");
      DSR(fr[0][0], la0, 24576);
      DSR(fr[0][1], la0, 25600);
      DSR(fr[0][2], la0, 26624);
      DSR(fr[0][3], la0, 27648);
      __builtin_amdgcn_sched_barrier(0);
      ac[0] = MFMA16(fr[1][0], xf[11], ac[0]);
      ac[1] = MFMA16(fr[1][1], xf[11], ac[1]);
      ac[2] = MFMA16(fr[1][2], xf[11], ac[2]);
      ac[3] = MFMA16(fr[1][3], xf[11], ac[3]);
      EPI_STEP(11, 0);
      __builtin_amdgcn_sched_barrier(0);
      asm volatile("s_waitcnt lgkmcnt(0)" ::: "memory");
      DSR(fr[1][0], la0, 28672);
      DSR(fr[1][1], la0, 29696);
      DSR(fr[1][2], la0, 30720);
      DSR(fr[1][3], la0, 31744);
      __builtin_amdgcn_sched_barrier(0);
      ac[0] = MFMA16(fr[0][0], xf[12], ac[0]);
      ac[1] = MFMA16(fr[0][1], xf[12], ac[1]);
      ac[2] = MFMA16(fr[0][2], xf[12], ac[2]);
      ac[3] = MFMA16(fr[0][3], xf[12], ac[3]);
      EPI_STEP(12, 0);
      __builtin_amdgcn_sched_barrier(0);
      asm volatile("s_waitcnt lgkmcnt(0)" ::: "memory");
      DSR(fr[0][0], la0, 32768);
      DSR(fr[0][1], la0, 33792);
      DSR(fr[0][2], la0, 34816);
      DSR(fr[0][3], la0, 35840);
      __builtin_amdgcn_sched_barrier(0);
      ac[0] = MFMA16(fr[1][0], xf[13], ac[0]);
      ac[1] = MFMA16(fr[1][1], xf[13], ac[1]);
      ac[2] = MFMA16(fr[1][2], xf[13], ac[2]);
      ac[3] = MFMA16(fr[1][3], xf[13], ac[3]);
      EPI_STEP(13, 0);
      __builtin_amdgcn_sched_barrier(0);
      EPI_PACK();
      asm volatile("s_waitcnt vmcnt(4)" ::: "memory");
      __builtin_amdgcn_s_barrier();
      G1_LOAD(11, 1);
      asm volatile("s_waitcnt lgkmcnt(0)" ::: "memory");
      DSR(fr[1][0], la0, 36864);
      DSR(fr[1][1], la0, 37888);
      DSR(fr[1][2], la0, 38912);
      DSR(fr[1][3], la0, 39936);
      __builtin_amdgcn_sched_barrier(0);
      ac[0] = MFMA16(fr[0][0], xf[0], zero);
      ac[1] = MFMA16(fr[0][1], xf[0], zero);
      ac[2] = MFMA16(fr[0][2], xf[0], zero);
      ac[3] = MFMA16(fr[0][3], xf[0], zero);
      EPI_STEP(0, 1);
      __builtin_amdgcn_sched_barrier(0);
      asm volatile("s_waitcnt lgkmcnt(0)" ::: "memory");
      DSR(fr[0][0], la0, 40960);
      DSR(fr[0][1], la0, 41984);
      DSR(fr[0][2], la0, 43008);
      DSR(fr[0][3], la0, 44032);
      __builtin_amdgcn_sched_barrier(0);
      ac[0] = MFMA16(fr[1][0], xf[1], ac[0]);
      ac[1] = MFMA16(fr[1][1], xf[1], ac[1]);
      ac[2] = MFMA16(fr[1][2], xf[1], ac[2]);
      ac[3] = MFMA16(fr[1][3], xf[1], ac[3]);
      EPI_STEP(1, 1);
      __builtin_amdgcn_sched_barrier(0);
      asm volatile("s_waitcnt lgkmcnt(0)" ::: "memory");
      DSR(fr[1][0], la0, 45056);
      DSR(fr[1][1], la0, 46080);
      DSR(fr[1][2], la0, 47104);
      DSR(fr[1][3], la0, 48128);
      __builtin_amdgcn_sched_barrier(0);
      ac[0] = MFMA16(fr[0][0], xf[2], ac[0]);
      ac[1] = MFMA16(fr[0][1], xf[2], ac[1]);
      ac[2] = MFMA16(fr[0][2], xf[2], ac[2]);
      ac[3] = MFMA16(fr[0][3], xf[2], ac[3]);
      EPI_STEP(2, 1);
      __builtin_amdgcn_sched_barrier(0);
      asm volatile("s_waitcnt lgkmcnt(0)" ::: "memory");
      DSR(fr[0][0], la0, 49152);
      DSR(fr[0][1], la0, 50176);
      DSR(fr[0][2], la0, 51200);
      DSR(fr[0][3], la0, 52224);
      __builtin_amdgcn_sched_barrier(0);
      ac[0] = MFMA16(fr[1][0], xf[3], ac[0]);
      ac[1] = MFMA16(fr[1][1], xf[3], ac[1]);
      ac[2] = MFMA16(fr[1][2], xf[3], ac[2]);
      ac[3] = MFMA16(fr[1][3], xf[3], ac[3]);
      EPI_STEP(3, 1);
      __builtin_amdgcn_sched_barrier(0);
      asm volatile("s_waitcnt vmcnt(4)" ::: "memory");
      __builtin_amdgcn_s_barrier();
      G1_LOAD(12, 2);
      asm volatile("s_waitcnt lgkmcnt(0)" ::: "memory");
      DSR(fr[1][0], la0, 53248);
      DSR(fr[1][1], la0, 54272);
      DSR(fr[1][2], la0, 55296);
      DSR(fr[1][3], la0, 56320);
      __builtin_amdgcn_sched_barrier(0);
      ac[0] = MFMA16(fr[0][0], xf[4], ac[0]);
      ac[1] = MFMA16(fr[0][1], xf[4], ac[1]);
      ac[2] = MFMA16(fr[0][2], xf[4], ac[2]);
      ac[3] = MFMA16(fr[0][3], xf[4], ac[3]);
      EPI_STEP(4, 1);
      __builtin_amdgcn_sched_barrier(0);
      asm volatile("s_waitcnt lgkmcnt(0)" ::: "memory");
      DSR(fr[0][0], la0, 57344);
      DSR(fr[0][1], la0, 58368);
      DSR(fr[0][2], la0, 59392);
      DSR(fr[0][3], la0, 60416);
      __builtin_amdgcn_sched_barrier(0);
      ac[0] = MFMA16(fr[1][0], xf[5], ac[0]);
      ac[1] = MFMA16(fr[1][1], xf[5], ac[1]);
      ac[2] = MFMA16(fr[1][2], xf[5], ac[2]);
      ac[3] = MFMA16(fr[1][3], xf[5], ac[3]);
      EPI_STEP(5, 1);
      __builtin_amdgcn_sched_barrier(0);
      asm volatile("s_waitcnt lgkmcnt(0)" ::: "memory");
      DSR(fr[1][0], la0, 61440);
      DSR(fr[1][1], la0, 62464);
      DSR(fr[1][2], la0, 63488);
      DSR(fr[1][3], la0, 64512);
      __builtin_amdgcn_sched_barrier(0);
      ac[0] = MFMA16(fr[0][0], xf[6], ac[0]);
      ac[1] = MFMA16(fr[0][1], xf[6], ac[1]);
      ac[2] = MFMA16(fr[0][2], xf[6], ac[2]);
      ac[3] = MFMA16(fr[0][3], xf[6], ac[3]);
      EPI_STEP(6, 1);
      __builtin_amdgcn_sched_barrier(0);
      asm volatile("s_waitcnt lgkmcnt(0)" ::: "memory");
      DSR(fr[0][0], la4, 0);
      DSR(fr[0][1], la4, 1024);
      DSR(fr[0][2], la4, 2048);
      DSR(fr[0][3], la4, 3072);
      __builtin_amdgcn_sched_barrier(0);
      ac[0] = MFMA16(fr[1][0], xf[7], ac[0]);
      ac[1] = MFMA16(fr[1][1], xf[7], ac[1]);
      ac[2] = MFMA16(fr[1][2], xf[7], ac[2]);
      ac[3] = MFMA16(fr[1][3], xf[7], ac[3]);
      EPI_STEP(7, 1);
      __builtin_amdgcn_sched_barrier(0);
      asm volatile("s_waitcnt vmcnt(4)" ::: "memory");
      __builtin_amdgcn_s_barrier();
      G1_LOAD(13, 3);
      asm volatile("s_waitcnt lgkmcnt(0)" ::: "memory");
      DSR(fr[1][0], la4, 4096);
      DSR(fr[1][1], la4, 5120);
      DSR(fr[1][2], la4, 6144);
      DSR(fr[1][3], la4, 7168);
      __builtin_amdgcn_sched_barrier(0);
      ac[0] = MFMA16(fr[0][0], xf[8], ac[0]);
      ac[1] = MFMA16(fr[0][1], xf[8], ac[1]);
      ac[2] = MFMA16(fr[0][2], xf[8], ac[2]);
      ac[3] = MFMA16(fr[0][3], xf[8], ac[3]);
      EPI_STEP(8, 1);
      __builtin_amdgcn_sched_barrier(0);
      asm volatile("s_waitcnt lgkmcnt(0)" ::: "memory");
      DSR(fr[0][0], la4, 8192);
      DSR(fr[0][1], la4, 9216);
      DSR(fr[0][2], la4, 10240);
      DSR(fr[0][3], la4, 11264);
      __builtin_amdgcn_sched_barrier(0);
      ac[0] = MFMA16(fr[1][0], xf[9], ac[0]);
      ac[1] = MFMA16(fr[1][1], xf[9], ac[1]);
      ac[2] = MFMA16(fr[1][2], xf[9], ac[2]);
      ac[3] = MFMA16(fr[1][3], xf[9], ac[3]);
      EPI_STEP(9, 1);
      __builtin_amdgcn_sched_barrier(0);
      asm volatile("s_waitcnt lgkmcnt(0)" ::: "memory");
      DSR(fr[1][0], la4, 12288);
      DSR(fr[1][1], la4, 13312);
      DSR(fr[1][2], la4, 14336);
      DSR(fr[1][3], la4, 15360);
      __builtin_amdgcn_sched_barrier(0);
      ac[0] = MFMA16(fr[0][0], xf[10], ac[0]);
      ac[1] = MFMA16(fr[0][1], xf[10], ac[1]);
      ac[2] = MFMA16(fr[0][2], xf[10], ac[2]);
      ac[3] = MFMA16(fr[0][3], xf[10], ac[3]);
      EPI_STEP(10, 1);
      __builtin_amdgcn_sched_barrier(0);
      asm volatile("s_waitcnt lgkmcnt(0)" ::: "memory");
      DSR(fr[0][0], la0, 0);
      DSR(fr[0][1], la0, 1024);
      DSR(fr[0][2], la0, 2048);
      DSR(fr[0][3], la0, 3072);
      __builtin_amdgcn_sched_barrier(0);
      ac[0] = MFMA16(fr[1][0], xf[11], ac[0]);
      ac[1] = MFMA16(fr[1][1], xf[11], ac[1]);
      ac[2] = MFMA16(fr[1][2], xf[11], ac[2]);
      ac[3] = MFMA16(fr[1][3], xf[11], ac[3]);
      EPI_STEP(11, 1);
      __builtin_amdgcn_sched_barrier(0);
      asm volatile("s_waitcnt vmcnt(4)" ::: "memory");
      __builtin_amdgcn_s_barrier();
      asm volatile("s_waitcnt lgkmcnt(0)" ::: "memory");
      DSR(fr[1][0], la0, 4096);
      DSR(fr[1][1], la0, 5120);
      DSR(fr[1][2], la0, 6144);
      DSR(fr[1][3], la0, 7168);
      __builtin_amdgcn_sched_barrier(0);
      ac[0] = MFMA16(fr[0][0], xf[12], ac[0]);
      ac[1] = MFMA16(fr[0][1], xf[12], ac[1]);
      ac[2] = MFMA16(fr[0][2], xf[12], ac[2]);
      ac[3] = MFMA16(fr[0][3], xf[12], ac[3]);
      EPI_STEP(12, 1);
      __builtin_amdgcn_sched_barrier(0);
      asm volatile("s_waitcnt lgkmcnt(0)" ::: "memory");
      DSR(fr[0][0], la0, 8192);
      DSR(fr[0][1], la0, 9216);
      DSR(fr[0][2], la0, 10240);
      DSR(fr[0][3], la0, 11264);
      __builtin_amdgcn_sched_barrier(0);
      ac[0] = MFMA16(fr[1][0], xf[13], ac[0]);
      ac[1] = MFMA16(fr[1][1], xf[13], ac[1]);
      ac[2] = MFMA16(fr[1][2], xf[13], ac[2]);
      ac[3] = MFMA16(fr[1][3], xf[13], ac[3]);
      EPI_STEP(13, 1);
      __builtin_amdgcn_sched_barrier(0);
      EPI_PACK();
      asm volatile("s_waitcnt lgkmcnt(0)" ::: "memory");
      DSR(fr[1][0], la0, 12288);
      DSR(fr[1][1], la0, 13312);
      DSR(fr[1][2], la0, 14336);
      DSR(fr[1][3], la0, 15360);
      __builtin_amdgcn_sched_barrier(0);
      ac[0] = MFMA16(fr[0][0], xf[0], zero);
      ac[1] = MFMA16(fr[0][1], xf[0], zero);
      ac[2] = MFMA16(fr[0][2], xf[0], zero);
      ac[3] = MFMA16(fr[0][3], xf[0], zero);
      EPI_STEP(0, 2);
      __builtin_amdgcn_sched_barrier(0);
      asm volatile("s_waitcnt lgkmcnt(0)" ::: "memory");
      DSR(fr[0][0], la0, 16384);
      DSR(fr[0][1], la0, 17408);
      DSR(fr[0][2], la0, 18432);
      DSR(fr[0][3], la0, 19456);
      __builtin_amdgcn_sched_barrier(0);
      ac[0] = MFMA16(fr[1][0], xf[1], ac[0]);
      ac[1] = MFMA16(fr[1][1], xf[1], ac[1]);
      ac[2] = MFMA16(fr[1][2], xf[1], ac[2]);
      ac[3] = MFMA16(fr[1][3], xf[1], ac[3]);
      EPI_STEP(1, 2);
      __builtin_amdgcn_sched_barrier(0);
      asm volatile("s_waitcnt vmcnt(2)" ::: "memory");
      __builtin_amdgcn_s_barrier();
      asm volatile("s_waitcnt lgkmcnt(0)" ::: "memory");
      DSR(fr[1][0], la0, 20480);
      DSR(fr[1][1], la0, 21504);
      DSR(fr[1][2], la0, 22528);
      DSR(fr[1][3], la0, 23552);
      __builtin_amdgcn_sched_barrier(0);
      ac[0] = MFMA16(fr[0][0], xf[2], ac[0]);
      ac[1] = MFMA16(fr[0][1], xf[2], ac[1]);
      ac[2] = MFMA16(fr[0][2], xf[2], ac[2]);
      ac[3] = MFMA16(fr[0][3], xf[2], ac[3]);
      EPI_STEP(2, 2);
      __builtin_amdgcn_sched_barrier(0);
      asm volatile("s_waitcnt lgkmcnt(0)" ::: "memory");
      DSR(fr[0][0], la0, 24576);
      DSR(fr[0][1], la0, 25600);
      DSR(fr[0][2], la0, 26624);
      DSR(fr[0][3], la0, 27648);
      __builtin_amdgcn_sched_barrier(0);
      ac[0] = MFMA16(fr[1][0], xf[3], ac[0]);
      ac[1] = MFMA16(fr[1][1], xf[3], ac[1]);
      ac[2] = MFMA16(fr[1][2], xf[3], ac[2]);
      ac[3] = MFMA16(fr[1][3], xf[3], ac[3]);
      EPI_STEP(3, 2);
      __builtin_amdgcn_sched_barrier(0);
      asm volatile("s_waitcnt lgkmcnt(0)" ::: "memory");
      DSR(fr[1][0], la0, 28672);
      DSR(fr[1][1], la0, 29696);
      DSR(fr[1][2], la0, 30720);
      DSR(fr[1][3], la0, 31744);
      __builtin_amdgcn_sched_barrier(0);
      ac[0] = MFMA16(fr[0][0], xf[4], ac[0]);
      ac[1] = MFMA16(fr[0][1], xf[4], ac[1]);
      ac[2] = MFMA16(fr[0][2], xf[4], ac[2]);
      ac[3] = MFMA16(fr[0][3], xf[4], ac[3]);
      EPI_STEP(4, 2);
      __builtin_amdgcn_sched_barrier(0);
      asm volatile("s_waitcnt lgkmcnt(0)" ::: "memory");
      DSR(fr[0][0], la0, 32768);
      DSR(fr[0][1], la0, 33792);
      DSR(fr[0][2], la0, 34816);
      DSR(fr[0][3], la0, 35840);
      __builtin_amdgcn_sched_barrier(0);
      ac[0] = MFMA16(fr[1][0], xf[5], ac[0]);
      ac[1] = MFMA16(fr[1][1], xf[5], ac[1]);
      ac[2] = MFMA16(fr[1][2], xf[5], ac[2]);
      ac[3] = MFMA16(fr[1][3], xf[5], ac[3]);
      EPI_STEP(5, 2);
      __builtin_amdgcn_sched_barrier(0);
      asm volatile("s_waitcnt vmcnt(0)" ::: "memory");
      __builtin_amdgcn_s_barrier();
      asm volatile("s_waitcnt lgkmcnt(0)" ::: "memory");
      DSR(fr[1][0], la0, 36864);
      DSR(fr[1][1], la0, 37888);
      DSR(fr[1][2], la0, 38912);
      DSR(fr[1][3], la0, 39936);
      __builtin_amdgcn_sched_barrier(0);
      ac[0] = MFMA16(fr[0][0], xf[6], ac[0]);
      ac[1] = MFMA16(fr[0][1], xf[6], ac[1]);
      ac[2] = MFMA16(fr[0][2], xf[6], ac[2]);
      ac[3] = MFMA16(fr[0][3], xf[6], ac[3]);
      EPI_STEP(6, 2);
      __builtin_amdgcn_sched_barrier(0);
      asm volatile("s_waitcnt lgkmcnt(0)" ::: "memory");
      DSR(fr[0][0], la0, 40960);
      DSR(fr[0][1], la0, 41984);
      DSR(fr[0][2], la0, 43008);
      DSR(fr[0][3], la0, 44032);
      __builtin_amdgcn_sched_barrier(0);
      ac[0] = MFMA16(fr[1][0], xf[7], ac[0]);
      ac[1] = MFMA16(fr[1][1], xf[7], ac[1]);
      ac[2] = MFMA16(fr[1][2], xf[7], ac[2]);
      ac[3] = MFMA16(fr[1][3], xf[7], ac[3]);
      EPI_STEP(7, 2);
      __builtin_amdgcn_sched_barrier(0);
      asm volatile("s_waitcnt lgkmcnt(0)" ::: "memory");
      DSR(fr[1][0], la0, 45056);
      DSR(fr[1][1], la0, 46080);
      DSR(fr[1][2], la0, 47104);
      DSR(fr[1][3], la0, 48128);
      __builtin_amdgcn_sched_barrier(0);
      ac[0] = MFMA16(fr[0][0], xf[8], ac[0]);
      ac[1] = MFMA16(fr[0][1], xf[8], ac[1]);
      ac[2] = MFMA16(fr[0][2], xf[8], ac[2]);
      ac[3] = MFMA16(fr[0][3], xf[8], ac[3]);
      EPI_STEP(8, 2);
      __builtin_amdgcn_sched_barrier(0);
      asm volatile("s_waitcnt lgkmcnt(0)" ::: "memory");
      DSR(fr[0][0], la0, 49152);
      DSR(fr[0][1], la0, 50176);
      DSR(fr[0][2], la0, 51200);
      DSR(fr[0][3], la0, 52224);
      __builtin_amdgcn_sched_barrier(0);
      ac[0] = MFMA16(fr[1][0], xf[9], ac[0]);
      ac[1] = MFMA16(fr[1][1], xf[9], ac[1]);
      ac[2] = MFMA16(fr[1][2], xf[9], ac[2]);
      ac[3] = MFMA16(fr[1][3], xf[9], ac[3]);
      EPI_STEP(9, 2);
      __builtin_amdgcn_sched_barrier(0);
      asm volatile("s_waitcnt vmcnt(0)" ::: "memory");
      __builtin_amdgcn_s_barrier();
      asm volatile("s_waitcnt lgkmcnt(0)" ::: "memory");
      DSR(fr[1][0], la0, 53248);
      DSR(fr[1][1], la0, 54272);
      DSR(fr[1][2], la0, 55296);
      DSR(fr[1][3], la0, 56320);
      __builtin_amdgcn_sched_barrier(0);
      ac[0] = MFMA16(fr[0][0], xf[10], ac[0]);
      ac[1] = MFMA16(fr[0][1], xf[10], ac[1]);
      ac[2] = MFMA16(fr[0][2], xf[10], ac[2]);
      ac[3] = MFMA16(fr[0][3], xf[10], ac[3]);
      EPI_STEP(10, 2);
      __builtin_amdgcn_sched_barrier(0);
      asm volatile("s_waitcnt lgkmcnt(0)" ::: "memory");
      DSR(fr[0][0], la0, 57344);
      DSR(fr[0][1], la0, 58368);
      DSR(fr[0][2], la0, 59392);
      DSR(fr[0][3], la0, 60416);
      __builtin_amdgcn_sched_barrier(0);
      ac[0] = MFMA16(fr[1][0], xf[11], ac[0]);
      ac[1] = MFMA16(fr[1][1], xf[11], ac[1]);
      ac[2] = MFMA16(fr[1][2], xf[11], ac[2]);
      ac[3] = MFMA16(fr[1][3], xf[11], ac[3]);
      EPI_STEP(11, 2);
      __builtin_amdgcn_sched_barrier(0);
      asm volatile("s_waitcnt lgkmcnt(0)" ::: "memory");
      DSR(fr[1][0], la0, 61440);
      DSR(fr[1][1], la0, 62464);
      DSR(fr[1][2], la0, 63488);
      DSR(fr[1][3], la0, 64512);
      __builtin_amdgcn_sched_barrier(0);
      ac[0] = MFMA16(fr[0][0], xf[12], ac[0]);
      ac[1] = MFMA16(fr[0][1], xf[12], ac[1]);
      ac[2] = MFMA16(fr[0][2], xf[12], ac[2]);
      ac[3] = MFMA16(fr[0][3], xf[12], ac[3]);
      EPI_STEP(12, 2);
      __builtin_amdgcn_sched_barrier(0);
      asm volatile("s_waitcnt lgkmcnt(0)" ::: "memory");
      __builtin_amdgcn_sched_barrier(0);
      ac[0] = MFMA16(fr[1][0], xf[13], ac[0]);
      ac[1] = MFMA16(fr[1][1], xf[13], ac[1]);
      ac[2] = MFMA16(fr[1][2], xf[13], ac[2]);
      ac[3] = MFMA16(fr[1][3], xf[13], ac[3]);
      EPI_STEP(13, 2);
      __builtin_amdgcn_sched_barrier(0);
      EPI_PACK();
    asm volatile("s_waitcnt lgkmcnt(0)" ::: "memory");
#pragma unroll
    for (int p = 0; p < 4; ++p) { EPI_A(p); EPI_B(p); EPI_C(p, 3); }
  } else if (b >= K1_G + K1_P) {
      const int e = (b - K1_G - K1_P) * 512 + tid, fbk = e / 56, db = e - fbk * 56;
      if (e >= 12544) return;
      u32x4 r[8], c[8];
      const int s = fbk >> 1, hh = fbk & 1;
      const u16* src = wd + (size_t)(32 * (s >> 1) + 8 * (s & 1) + 4 * hh) * D_ + 8 * db;
#pragma unroll
      for (int j = 0; j < 8; ++j) r[j] = *(const u32x4*)(src + (size_t)(16 * (j >> 2) + (j & 3)) * D_);
      transpose8x8(r, c);
      const int dt = db >> 2, dt64 = dt >> 1, dtl = dt & 1, hi = (db >> 1) & 1, q0 = 2 * (db & 1);
      u32x4* dst = (u32x4*)(ws + OFF_WD) + (((size_t)(dt64 * 112 + s) * 2 + dtl) * 64 + hh * 32 + 4 * hi);
#pragma unroll
      for (int dd = 0; dd < 8; ++dd) dst[8 * (q0 + (dd >> 2)) + (dd & 3)] = c[dd];
  } else {
    if (w >= 4) return;
    const int half = 0, t = tid;
    const int k2 = b - K1_G;
    const bool valid = true;
    const int k2c = k2;
    f32x2* col = (f32x2*)(ws + OFF_XT) + (size_t)k2c * S_;
    const u32x4* wt = (const u32x4*)(ws + OFF_WT + ((size_t)k2c * S_ + 8 * t) * 4);
    f32x2* E = (f32x2*)(smem + half * 20480);
    f32x2 v[8];
#pragma unroll
    for (int j = 0; j < 8; ++j) v[j] = col[t + 256 * j];
    const u32x4 w0 = wt[0], w1 = wt[1];
    const int q = t >> 5, t1 = t & 31, q2 = t1 >> 2, t1a = t & 3;
    f32x2 tA[8], tB[8], tC[8];
#pragma unroll
    for (int j = 1; j < 8; ++j) { tA[j] = tw2048(t * j); tB[j] = tw2048(8 * t1 * j); tC[j] = tw2048(64 * t1a * j); }
    f32x2* R = E + q * 288;
    STAMP(1, 2);
    dft8<false>(v);
#pragma unroll
    for (int j = 1; j < 8; ++j) v[j] = cmul(v[j], tA[j]);
#pragma unroll
    for (int j = 0; j < 8; ++j) E[j * 288 + t] = v[j];
    block_sync();
#pragma unroll
    for (int j = 0; j < 8; ++j) v[j] = R[t1 + 32 * j];
    dft8<false>(v);
#pragma unroll
    for (int j = 1; j < 8; ++j) v[j] = cmul(v[j], tB[j]);
    wave_lds_fence();
#pragma unroll
    for (int j = 0; j < 8; ++j) R[j * 36 + t1] = v[j];
    wave_lds_fence();
#pragma unroll
    for (int j = 0; j < 8; ++j) v[j] = R[q2 * 36 + t1a + 4 * j];
    dft8<false>(v);
#pragma unroll
    for (int j = 1; j < 8; ++j) v[j] = cmul(v[j], tC[j]);
    wave_lds_fence();
#pragma unroll
    for (int j = 0; j < 8; ++j) R[q2 * 36 + 4 * j + t1a] = v[j];
    wave_lds_fence();
#pragma unroll
    for (int j = 0; j < 8; ++j) v[j] = R[q2 * 36 + 8 * t1a + j];
    dft4<false>(v); dft4<false>(v + 4);
    const float sc = 1.f / (2048.f * 448.f);
#pragma unroll
    for (int j = 0; j < 4; ++j) {
      v[j] = cmul(v[j], f32x2{bflo(w0[j]) * sc, bfhi(w0[j]) * sc});
      v[4 + j] = cmul(v[4 + j], f32x2{bflo(w1[j]) * sc, bfhi(w1[j]) * sc});
    }
    dft4<true>(v); dft4<true>(v + 4);
    wave_lds_fence();
#pragma unroll
    for (int j = 0; j < 8; ++j) R[q2 * 36 + 8 * t1a + j] = v[j];
    wave_lds_fence();
#pragma unroll
    for (int j = 0; j < 8; ++j) v[j] = R[q2 * 36 + 4 * j + t1a];
#pragma unroll
    for (int j = 1; j < 8; ++j) v[j] = cmulc(v[j], tC[j]);
    dft8<true>(v);
    wave_lds_fence();
#pragma unroll
    for (int j = 0; j < 8; ++j) R[q2 * 36 + t1a + 4 * j] = v[j];
    wave_lds_fence();
#pragma unroll
    for (int j = 0; j < 8; ++j) v[j] = R[j * 36 + t1];
#pragma unroll
    for (int j = 1; j < 8; ++j) v[j] = cmulc(v[j], tB[j]);
    dft8<true>(v);
    wave_lds_fence();
#pragma unroll
    for (int j = 0; j < 8; ++j) R[t1 + 32 * j] = v[j];
    block_sync();
#pragma unroll
    for (int j = 0; j < 8; ++j) v[j] = E[j * 288 + t];
#pragma unroll
    for (int j = 1; j < 8; ++j) v[j] = cmulc(v[j], tA[j]);
    dft8<true>(v);
    STAMP(1, 3);
    if (valid) {
#pragma unroll
      for (int j = 0; j < 8; ++j) col[t + 256 * j] = v[j];
    }
  }
  STAMP(1, 1);
}

constexpr int K2_P = 256, K2_G = 224;
typedef __attribute__((address_space(1))) unsigned gu32;
#define RLX_AGENT __ATOMIC_RELAXED, __HIP_MEMORY_SCOPE_AGENT
__global__ NOPK __launch_bounds__(256, 2) void k2_gemm2_rowifft(const u16* __restrict__ x, char* __restrict__ ws, u16* __restrict__ out) {
  __shared__ __attribute__((aligned(16))) char smem[65536 + 1024];
  const int tid = __builtin_amdgcn_workitem_id_x(), w = __builtin_amdgcn_readfirstlane(tid >> 6), lane = tid & 63;
  const int b = __builtin_amdgcn_workgroup_id_x();
  STAMP(2, 0);
  code_prefetch<PF2>(smem + 65536, w, lane);
  if (b < K2_P) {
    f32x2* zb = (f32x2*)(smem + w * 4096);
    f32x2* tt = (f32x2*)(smem + 4 * 4096);
    const int r0 = 8 * b, ra = r0 + 2 * w;
    const f32x2* ZT = (const f32x2*)(ws + OFF_XT);
    const bool act = lane < 56;
    const int k1 = act ? (lane >> 3) : 0, a = lane & 7;
    f32x4 zin[4];
#pragma unroll
    for (int ii = 0; ii < 4; ++ii) {
      const int e = tid + 256 * ii;
      if (e < DF * 4) { const int k = e >> 2, p = e & 3; zin[ii] = *(const f32x4*)(ZT + (size_t)k * S_ + r0 + 2 * p); }
    }
    u32x4 xa = {0, 0, 0, 0}, xb = {0, 0, 0, 0};
    if (act) { xa = *(const u32x4*)(x + (size_t)ra * D_ + lane * 8); xb = *(const u32x4*)(x + (size_t)(ra + 1) * D_ + lane * 8); }
    f32x2 tA[8], tB[8];
#pragma unroll
    for (int c = 1; c < 8; ++c) tA[c] = tw448(7 * a * c);
#pragma unroll
    for (int kk = 1; kk < 7; ++kk) tB[kk] = tw448(lane * kk);
#pragma unroll
    for (int ii = 0; ii < 4; ++ii) {
      const int e = tid + 256 * ii;
      if (e < DF * 4) { const int k = e >> 2, p = e & 3; *(f32x4*)(tt + k * 8 + 2 * p) = zin[ii]; }
    }
    block_sync();
    STAMP(2, 2);
#pragma unroll
    for (int ii = 0; ii < 4; ++ii) {
      const int k = lane + 64 * ii;
      if (k < DF) {
        f32x4 ab = *(const f32x4*)(tt + k * 8 + 2 * w);
        if (k == 0 || k == 224) { ab.y = 0.f; ab.w = 0.f; }
        zb[k] = f32x2{ab.x - ab.w, ab.y + ab.z};
        if (k != 0 && k != 224) zb[448 - k] = f32x2{ab.x + ab.w, ab.z - ab.y};
      }
    }
    wave_lds_fence();
    f32x2 v[8];
#pragma unroll
    for (int d = 0; d < 8; ++d) v[d] = zb[k1 + 7 * a + 56 * d];
    dft8<true>(v);
    wave_lds_fence();
    if (act) {
#pragma unroll
      for (int aa = 0; aa < 8; ++aa) zb[72 * k1 + 9 * aa + a] = v[aa];
    }
    wave_lds_fence();
#pragma unroll
    for (int c = 0; c < 8; ++c) v[c] = zb[72 * k1 + 9 * a + c];
#pragma unroll
    for (int c = 1; c < 8; ++c) v[c] = cmulc(v[c], tA[c]);
    dft8<true>(v);
    wave_lds_fence();
    if (act) {
#pragma unroll
      for (int bb = 0; bb < 8; ++bb) zb[72 * k1 + a + 8 * bb] = v[bb];
    }
    wave_lds_fence();
#pragma unroll
    for (int kk = 0; kk < 7; ++kk) v[kk] = zb[72 * kk + lane];
#pragma unroll
    for (int kk = 1; kk < 7; ++kk) v[kk] = cmulc(v[kk], tB[kk]);
    dft7<true>(v);
    wave_lds_fence();
#pragma unroll
    for (int n1 = 0; n1 < 7; ++n1) zb[64 * n1 + lane] = v[n1];
    wave_lds_fence();
    const auto rs = __builtin_amdgcn_make_buffer_rsrc(ws, 0, 0x10000000, 0x00020000);
    if (act) {
      u32x4 oa, ob;
#pragma unroll
      for (int j = 0; j < 4; ++j) {
        f32x4 z2 = *(const f32x4*)(zb + 8 * lane + 2 * j);
        u32 ya = cvtpk(z2.x, z2.z), yb = cvtpk(z2.y, z2.w);
        oa[j] = cvtpk(bflo(xa[j]) + bflo(ya), bfhi(xa[j]) + bfhi(ya));
        ob[j] = cvtpk(bflo(xb[j]) + bflo(yb), bfhi(xb[j]) + bfhi(yb));
      }
      const unsigned off = (unsigned)OFF_T + (unsigned)(ra * D_ + lane * 8) * 2u;
      __builtin_amdgcn_raw_buffer_store_b128(oa, rs, off, 0, 16);
      __builtin_amdgcn_raw_buffer_store_b128(ob, rs, off + D_ * 2, 0, 16);
    }
    asm volatile("s_waitcnt vmcnt(0)" ::: "memory");
    block_sync();
    if (tid == 0) __hip_atomic_fetch_add((gu32*)(ws + OFF_FLG) + (b >> 3) * 16, 1u, RLX_AGENT);
  } else {
    const int g = b - K2_P, xcd = g & 7, i = g >> 3;
    const int rt64 = 4 * xcd + (i & 3), dt64 = i >> 2;
    const char* hsrc = ws + OFF_HT + ((size_t)(2 * rt64) * 112 + 28 * w) * 1024;
    const char* wsrc = ws + OFF_WD + ((size_t)(dt64 * 112 + 28 * w) * 2) * 1024;
    const unsigned vo16 = lane * 16;
    constexpr int RD = 6;
    bf16x8 hb[RD][2], wb[RD][2];
#pragma unroll
    for (int s = 0; s < RD; ++s) {
#pragma unroll
      for (int r = 0; r < 2; ++r) { wb[s][r] = *(const bf16x8*)(wsrc + (s * 2 + r) * 1024 + vo16); hb[s][r] = *(const bf16x8*)(hsrc + (r * 112 + s) * 1024 + vo16); }
    }
    f32x16 acc[2][2];
    const f32x16 zero = {0.f, 0.f, 0.f, 0.f, 0.f, 0.f, 0.f, 0.f, 0.f, 0.f, 0.f, 0.f, 0.f, 0.f, 0.f, 0.f};
#pragma unroll
    for (int s = 0; s < 28; ++s) {
#pragma unroll
      for (int d = 0; d < 2; ++d)
#pragma unroll
        for (int r = 0; r < 2; ++r) acc[d][r] = MFMA32(wb[s % RD][d], hb[s % RD][r], s == 0 ? zero : acc[d][r]);
      if (s + RD < 28) {
#pragma unroll
        for (int r = 0; r < 2; ++r) { wb[s % RD][r] = *(const bf16x8*)(wsrc + ((s + RD) * 2 + r) * 1024 + vo16); hb[s % RD][r] = *(const bf16x8*)(hsrc + (r * 112 + s + RD) * 1024 + vo16); }
      }
      __builtin_amdgcn_sched_barrier(0);
    }
    STAMP(2, 3);
    const int rr = w >> 1, dd = w & 1;
    const int row = 64 * rt64 + 32 * rr + (lane & 31), d0 = 64 * dt64 + 32 * dd + 16 * (lane >> 5);
    {
      gu32* f = (gu32*)(ws + OFF_FLG) + rt64 * 16;
      for (unsigned it_ = 0; it_ < (1u << 22); ++it_) {
        if (__builtin_amdgcn_readfirstlane(__hip_atomic_load(f, RLX_AGENT)) >= 8) break;
        __builtin_amdgcn_s_sleep(2);
      }
      asm volatile("" ::: "memory");
    }
    const auto rs2 = __builtin_amdgcn_make_buffer_rsrc(ws, 0, 0x10000000, 0x00020000);
    const unsigned to = (unsigned)OFF_T + (unsigned)(row * D_ + d0) * 2u;
    const u32x4 tv0 = __builtin_amdgcn_raw_buffer_load_b128(rs2, to, 0, 16);
    const u32x4 tv1 = __builtin_amdgcn_raw_buffer_load_b128(rs2, to + 16, 0, 16);
    f32x4* R = (f32x4*)smem;
#pragma unroll
    for (int r = 0; r < 2; ++r)
#pragma unroll
      for (int d = 0; d < 2; ++d)
#pragma unroll
        for (int qd = 0; qd < 4; ++qd)
          R[(((w * 2 + r) * 2 + d) * 4 + qd) * 64 + lane] = f32x4{acc[d][r][4 * qd], acc[d][r][4 * qd + 1], acc[d][r][4 * qd + 2], acc[d][r][4 * qd + 3]};
    block_sync();
    STAMP(2, 4);
    u32x4 o[2];
#pragma unroll
    for (int qd = 0; qd < 4; ++qd) {
      f32x4 s0 = {0.f, 0.f, 0.f, 0.f};
#pragma unroll
      for (int ww = 0; ww < 4; ++ww) s0 += R[(((ww * 2 + rr) * 2 + dd) * 4 + qd) * 64 + lane];
      const u32 y0 = cvtpk(s0[0], s0[1]), y1 = cvtpk(s0[2], s0[3]);
      const u32 t0 = (qd < 2 ? tv0 : tv1)[2 * (qd & 1)], t1 = (qd < 2 ? tv0 : tv1)[2 * (qd & 1) + 1];
      o[qd >> 1][2 * (qd & 1)] = cvtpk(bflo(t0) + bflo(y0), bfhi(t0) + bfhi(y0));
      o[qd >> 1][2 * (qd & 1) + 1] = cvtpk(bflo(t1) + bflo(y1), bfhi(t1) + bfhi(y1));
    }
    *(u32x4*)(out + (size_t)row * D_ + d0) = o[0];
    *(u32x4*)(out + (size_t)row * D_ + d0 + 8) = o[1];
  }
  STAMP(2, 1);
}

extern "C" void kernel_launch(void* const* d_in, const int* in_sizes, int n_in, void* d_out, int out_size, void* d_ws, size_t ws_size,
                              hipStream_t stream) {
  const u16* x = (const u16*)d_in[0];
  const u16* wr = (const u16*)d_in[1];
  const u16* wi = (const u16*)d_in[2];
  const u16* wg = (const u16*)d_in[4];
  const u16* wu = (const u16*)d_in[5];
  const u16* wd = (const u16*)d_in[6];
  char* ws = (char*)d_ws;
  k0_prep<<<K0_LN + K0_W + K0_T, 256, 0, stream>>>(x, wr, wi, wg, wu, ws);
  k1_gemm1_colfft<<<K1_G + K1_P + 25, 512, 0, stream>>>(wd, ws);
  k2_gemm2_rowifft<<<K2_P + K2_G, 256, 0, stream>>>(x, ws, (u16*)d_out);
}
```
